```python
import torch

def _softmax(x, axis=-1):
    return torch.softmax(x, dim=axis)
def _gelu(x):
    return torch.nn.functional.gelu(x, approximate="tanh")
def _silu(x):
    return torch.nn.functional.silu(x)
def _sigmoid(x):
    return torch.sigmoid(x)
def _layernorm(x, axis=-1, eps=1e-5):
    m = x.mean(dim=axis, keepdim=True)
    v = x.var(dim=axis, keepdim=True, unbiased=False)
    return (x - m) / torch.sqrt(v + eps)
def _scatter_add(tgt, idx, src, axis=0):
    return tgt.clone().index_add_(axis, idx, src)
def _segment_sum(src, seg, n):
    out = torch.zeros((n,) + src.shape[1:], dtype=src.dtype)
    return out.index_add_(0, seg, src)
def _pad(x, pw):
    flat = tuple(v for pair in reversed(pw) for v in pair)
    return torch.nn.functional.pad(x, flat)

kernel_name = 'standalone__paged_block_table_kv_pagedattention__sampling'

def setup_inputs():

    _w = (lambda a: (lambda t: t.to(torch.bfloat16) if t.is_floating_point() else t)(torch.as_tensor(a)))
    B, Hq, Hkv, Sq, Skv, D = 8, 32, 4, 32, 8192, 128
    bs = 16
    nbps = max(1, Skv // bs)
    num_phys = B * nbps + 4
    bt = torch.randperm(num_phys)[: B * nbps].reshape(B, nbps).to(torch.int64)
    cl = (nbps * bs - (torch.arange(1, B + 1, dtype=torch.int64) * 2654435761) % max(1, nbps * bs // 2)).to(torch.int64)
    return {
        "q": _w((lambda *s: torch.randn(*s, dtype=torch.bfloat16) * 0.8)(B, Hq, Sq, D)),
        "k_cache": _w((lambda *s: torch.randn(*s, dtype=torch.bfloat16) * 0.8)(num_phys, bs, Hkv, D)),
        "v_cache": _w((lambda *s: torch.randn(*s, dtype=torch.bfloat16) * 0.05)(num_phys, bs, Hkv, D)),
        "block_table": _w(bt),
        "context_lens": _w(cl),
    }

def reference(q, k_cache, v_cache, block_table, context_lens):
    B, Hq, Sq, D = q.shape
    bs = k_cache.shape[1]
    Hkv = k_cache.shape[2]
    nbps = block_table.shape[1]
    Skv = nbps * bs
    bt = block_table.reshape(-1)
    k = (lambda x, *d: x.permute(*d))(k_cache[bt].reshape(B, Skv, Hkv, D), 0, 2, 1, 3)
    v = (lambda x, *d: x.permute(*d))(v_cache[bt].reshape(B, Skv, Hkv, D), 0, 2, 1, 3)
    g = Hq // Hkv
    qg = q.reshape(B, Hkv, g, Sq, D)
    scores = torch.einsum("bhgqd,bhkd->bhgqk", qg, k) * (D ** -0.5)
    pos = torch.arange(Skv)
    msk = pos[None, :] < context_lens[:, None]
    scores = torch.where(msk[:, None, None, None, :], scores, -1e9)
    p = _softmax(scores, axis=-1)
    out = torch.einsum("bhgqk,bhkd->bhgqd", p, v)
    return out.reshape(B, Hq, Sq, D)
```

```cpp
#include <hip/hip_runtime.h>
#include <hip/hip_bf16.h>
#include <cstdio>

constexpr int B = 8, HQ = 32, HKV = 4, SQ = 32, D = 128, PAGE = 16, NBPS = 512, SKV = NBPS * PAGE;
constexpr int G = HQ / HKV, ROWS = G * SQ;
constexpr int KVBLK = 64;
constexpr int NSLOT = 64;
constexpr float SCALE = 0.088388347648318447f;
constexpr float L2E = 1.4426950408889634f, SL2E = SCALE * L2E;
constexpr float THR2 = 8.f;

using bf16 = __hip_bfloat16;
using bf16x8 = __attribute__((ext_vector_type(8))) short;
using s16x4 = __attribute__((ext_vector_type(4))) short;
using f32x16 = __attribute__((ext_vector_type(16))) float;
using f32x4 = __attribute__((ext_vector_type(4))) float;
using u32x4 = __attribute__((ext_vector_type(4))) unsigned;
using u32x2 = __attribute__((ext_vector_type(2))) unsigned;

#define KSWZ(row, colB) ((row) * 256 + ((colB) ^ (((row) & 7) << 4)))
#define SBAR() __builtin_amdgcn_sched_barrier(0)
__device__ __forceinline__ int crow(int r, int hi) { return (r & 3) + 8 * (r >> 2) + 4 * hi; }
__device__ __forceinline__ unsigned cvtpk(float lo, float hi) { unsigned r; asm volatile("v_cvt_pk_bf16_f32 %0, %1, %2" : "=v"(r) : "v"(lo), "v"(hi)); return r; }
__device__ __forceinline__ int v_st(int k, int c) { const int kk = (k & ~0xC) | ((k & 4) << 1) | ((k & 8) >> 1); return ((kk >> 3) * 4 + (c >> 5)) * 512 + ((kk & 7) * 32 + (c & 31)) * 2; }
__device__ __forceinline__ int v_rd_base(int lane) { return ((lane & 3) << 3) | (((lane >> 2) & 3) << 6) | (((lane >> 4) & 1) << 5) | (((lane >> 5) & 1) << 8); }
constexpr int v_rd_off(int d0, int ks, int half) { return d0 * 512 + ks * 4096 + half * 2048; }
template <int OFF> __device__ __forceinline__ s16x4 tr_read(int vb) {
  s16x4 r; asm volatile("ds_read_b64_tr_b16 %0, %1 offset:%2" : "=&v"(r) : "v"(vb), "i"(OFF) : "memory"); return r;
}
template <int D0> __device__ __forceinline__ void pv_one(f32x16& od, int vb, bf16x8 pa0, bf16x8 pa1, bf16x8 pa2, bf16x8 pa3) {
  const s16x4 l0 = tr_read<v_rd_off(D0, 0, 0)>(vb), h0 = tr_read<v_rd_off(D0, 0, 1)>(vb), l1 = tr_read<v_rd_off(D0, 1, 0)>(vb), h1 = tr_read<v_rd_off(D0, 1, 1)>(vb);
  const s16x4 l2 = tr_read<v_rd_off(D0, 2, 0)>(vb), h2 = tr_read<v_rd_off(D0, 2, 1)>(vb), l3 = tr_read<v_rd_off(D0, 3, 0)>(vb), h3 = tr_read<v_rd_off(D0, 3, 1)>(vb);
  asm volatile("s_waitcnt lgkmcnt(0)" ::: "memory"); SBAR();
#define PK(L, H) (bf16x8){L[0], L[1], L[2], L[3], H[0], H[1], H[2], H[3]}
  od = __builtin_amdgcn_mfma_f32_32x32x16_bf16(pa0, PK(l0, h0), od, 0, 0, 0);
  od = __builtin_amdgcn_mfma_f32_32x32x16_bf16(pa1, PK(l1, h1), od, 0, 0, 0);
  od = __builtin_amdgcn_mfma_f32_32x32x16_bf16(pa2, PK(l2, h2), od, 0, 0, 0);
  od = __builtin_amdgcn_mfma_f32_32x32x16_bf16(pa3, PK(l3, h3), od, 0, 0, 0);
#undef PK
}

struct Part { int b, s, nb, tb, start; };
__device__ __forceinline__ int eff_len(int cl) { return cl <= 0 ? SKV : (cl > SKV ? SKV : cl); }
__device__ __forceinline__ void partition(const int* lens, int slot, Part& p, int* nb_out  , int* st_out  ) {
  int T[B];
#pragma unroll
  for (int i = 0; i < B; ++i) T[i] = (eff_len(lens[i]) + KVBLK - 1) / KVBLK;
  int C = 16;
  for (int c = 1; c <= 16; ++c) { int sum = 0;
#pragma unroll
    for (int i = 0; i < B; ++i) sum += (T[i] + c - 1) / c;
    if (sum <= NSLOT) { C = c; break; } }
  int acc = 0; p.b = -1; p.s = 0; p.nb = 1; p.tb = 0; p.start = 0;
#pragma unroll
  for (int i = 0; i < B; ++i) { int n = (T[i] + C - 1) / C; nb_out[i] = n; st_out[i] = acc;
    if (slot >= acc && slot < acc + n) { p.b = i; p.s = slot - acc; p.nb = n; p.tb = T[i]; p.start = acc; }
    acc += n; }
}

constexpr size_t WS_PML = 0, WS_PO = (size_t)HKV * NSLOT * ROWS * 2 * 4;

__global__ __launch_bounds__(512, 2) void attn_split(const bf16* __restrict__ Q, const bf16* __restrict__ Kc, const bf16* __restrict__ Vc,
                                                      const int* __restrict__ bt, const int* __restrict__ lens, float* __restrict__ pml, float* __restrict__ po) {
  __shared__ __attribute__((aligned(16))) char shm[2 * 16384 + 8 * 256];
  const int tid = threadIdx.x, wid = tid >> 6, lane = tid & 63, r32 = lane & 31, hi = lane >> 5;
  const int g = blockIdx.x, x = g & 7, kvh = x & 3, slot = (x >> 2) * 32 + (g >> 3);
  Part p; int nbs[B], sts[B]; partition(lens, slot, p, nbs, sts);
  if (p.b < 0) return;
  const int b = p.b, clraw = lens[b], len = eff_len(clraw);
  const float sl2e = clraw <= 0 ? 0.f : SL2E;
  const int t0 = (p.s * p.tb) / p.nb, t1 = ((p.s + 1) * p.tb) / p.nb;
  char* V_lds = shm; char* K_lds = shm + 16384;
  float* al_l = (float*)(shm + 32768) + wid * 64;
  const bf16* Qg = Q + ((size_t)b * HQ + (size_t)kvh * G) * SQ * D;
  const int R = wid * 32 + r32;
  bf16x8 qr[8];
#pragma unroll
  for (int d0 = 0; d0 < 8; ++d0) qr[d0] = *reinterpret_cast<const bf16x8*>(Qg + (size_t)R * D + d0 * 16 + hi * 8);
  float m_reg = -__builtin_inff(), msub = 0.f, l_reg = 0.f; f32x16 o[4] = {};
  const int sr = tid >> 4, sc = (tid & 15) * 8;
  const int vst0 = v_st(sr, sc), vst1 = v_st(32 + sr, sc);
  const int vb0 = (int)(uintptr_t)V_lds + v_rd_base(lane);
  const int* btb = bt + b * NBPS;
  bf16x8 sv0, sv1, sk0, sk1;
  auto sload = [&](int t) {
    const int pg0 = btb[t * 4 + (sr >> 4)], pg1 = btb[t * 4 + 2 + (sr >> 4)];
    const size_t o0 = ((size_t)pg0 * PAGE + (sr & 15)) * (HKV * D) + kvh * D + sc, o1 = ((size_t)pg1 * PAGE + (sr & 15)) * (HKV * D) + kvh * D + sc;
    sk0 = *reinterpret_cast<const bf16x8*>(Kc + o0); sk1 = *reinterpret_cast<const bf16x8*>(Kc + o1);
    sv0 = *reinterpret_cast<const bf16x8*>(Vc + o0); sv1 = *reinterpret_cast<const bf16x8*>(Vc + o1);
  };
  sload(t0);
  for (int t = t0; t < t1; ++t) {
    __syncthreads();
    {
      const int k0 = t * KVBLK; bf16x8 z = {};
      bf16x8 a0 = sv0, a1 = sv1;
      if (k0 + sr >= len) a0 = z; if (k0 + 32 + sr >= len) a1 = z;
      *(bf16x8*)(V_lds + vst0) = a0; *(bf16x8*)(V_lds + vst1) = a1;
      *(bf16x8*)(K_lds + KSWZ(sr, sc * 2)) = sk0; *(bf16x8*)(K_lds + KSWZ(32 + sr, sc * 2)) = sk1;
    }
    __syncthreads();
    if (t + 1 < t1) sload(t + 1);
    f32x16 p0 = {}, p1 = {};
#pragma unroll
    for (int d0 = 0; d0 < 8; ++d0) { int cb = (d0 * 16 + hi * 8) * 2;
      bf16x8 b0 = *reinterpret_cast<const bf16x8*>(K_lds + KSWZ(r32, cb));
      bf16x8 b1 = *reinterpret_cast<const bf16x8*>(K_lds + KSWZ(32 + r32, cb));
      p0 = __builtin_amdgcn_mfma_f32_32x32x16_bf16(b0, qr[d0], p0, 0, 0, 0);
      p1 = __builtin_amdgcn_mfma_f32_32x32x16_bf16(b1, qr[d0], p1, 0, 0, 0); }
    const int kbase = t * KVBLK;
    if (kbase + KVBLK > len) { const float ninf = -__builtin_inff();
#pragma unroll
      for (int r = 0; r < 16; ++r) { const int k = kbase + crow(r, hi); p0[r] = k >= len ? ninf : p0[r]; p1[r] = k + 32 >= len ? ninf : p1[r]; } }
    float pmax = p0[0];
#pragma unroll
    for (int r = 1; r < 16; ++r) pmax = fmaxf(pmax, p0[r]);
#pragma unroll
    for (int r = 0; r < 16; ++r) pmax = fmaxf(pmax, p1[r]);
    { auto rr = __builtin_amdgcn_permlane32_swap(__float_as_uint(pmax), __float_as_uint(pmax), false, false);
      pmax = fmaxf(__uint_as_float(rr[0]), __uint_as_float(rr[1])); }
    pmax *= sl2e;
    float alpha;
    if (__builtin_expect(__all(pmax - m_reg <= THR2), 1)) { alpha = 1.f; }
    else { float mn = fmaxf(m_reg, pmax); msub = mn; alpha = __builtin_amdgcn_exp2f(m_reg - msub); m_reg = mn; }
    const float nm = -msub;
#pragma unroll
    for (int r = 0; r < 16; ++r) { p0[r] = __builtin_amdgcn_exp2f(fmaf(p0[r], sl2e, nm)); p1[r] = __builtin_amdgcn_exp2f(fmaf(p1[r], sl2e, nm)); }
    float ps = 0;
#pragma unroll
    for (int r = 0; r < 16; ++r) ps += p0[r] + p1[r];
    { auto rr = __builtin_amdgcn_permlane32_swap(__float_as_uint(ps), __float_as_uint(ps), false, false); ps = __uint_as_float(rr[0]) + __uint_as_float(rr[1]); }
    l_reg = l_reg * alpha + ps;
    bf16x8 pa0, pa1, pa2, pa3;
#define PK4(P, BASE, OUT) do { unsigned a0 = cvtpk(P[BASE + 0], P[BASE + 1]), a1 = cvtpk(P[BASE + 2], P[BASE + 3]); \
    unsigned b0 = cvtpk(P[BASE + 4], P[BASE + 5]), b1 = cvtpk(P[BASE + 6], P[BASE + 7]); \
    auto r0 = __builtin_amdgcn_permlane32_swap(a0, b0, false, false); auto r1 = __builtin_amdgcn_permlane32_swap(a1, b1, false, false); \
    u32x4 w = {r0[0], r1[0], r0[1], r1[1]}; OUT = *reinterpret_cast<bf16x8*>(&w); } while (0)
    PK4(p0, 0, pa0); PK4(p0, 8, pa1); PK4(p1, 0, pa2); PK4(p1, 8, pa3);
#undef PK4
    if (__any(alpha < 1.f)) { if (hi == 0) al_l[r32] = alpha; asm volatile("s_waitcnt lgkmcnt(0)" ::: "memory");
#pragma unroll
      for (int d = 0; d < 4; ++d)
#pragma unroll
        for (int r = 0; r < 16; ++r) o[d][r] *= al_l[crow(r, hi)]; }
    pv_one<0>(o[0], vb0, pa0, pa1, pa2, pa3); pv_one<1>(o[1], vb0, pa0, pa1, pa2, pa3); pv_one<2>(o[2], vb0, pa0, pa1, pa2, pa3); pv_one<3>(o[3], vb0, pa0, pa1, pa2, pa3);
  }
  const size_t pbase = ((size_t)kvh * NSLOT + slot) * ROWS;
  if (hi == 0) { float* pp = pml + (pbase + R) * 2; pp[0] = m_reg; pp[1] = l_reg; }
#pragma unroll
  for (int r = 0; r < 16; ++r) { const int Ro = wid * 32 + crow(r, hi); float* prow = po + (pbase + Ro) * D;
#pragma unroll
    for (int d0 = 0; d0 < 4; ++d0) prow[d0 * 32 + r32] = o[d0][r]; }
}

__global__ __launch_bounds__(256) void attn_combine(const int* __restrict__ lens, const float* __restrict__ pml, const float* __restrict__ po, bf16* __restrict__ O) {
  const int row = blockIdx.x * 8 + (threadIdx.x >> 5), d = (threadIdx.x & 31) * 4;
  const int b = row / (HQ * SQ), rem = row % (HQ * SQ), kvh = rem / ROWS, R = rem % ROWS;
  Part p; int nbs[B], sts[B]; partition(lens, 0, p, nbs, sts);
  int n = 0, st = 0;
#pragma unroll
  for (int i = 0; i < B; ++i) if (i == b) { n = nbs[i]; st = sts[i]; }
  const size_t base = ((size_t)kvh * NSLOT + st) * ROWS + R;
  float M = -__builtin_inff();
  for (int s = 0; s < n; ++s) M = fmaxf(M, pml[(base + (size_t)s * ROWS) * 2]);
  float L = 0.f; f32x4 o = {0.f, 0.f, 0.f, 0.f};
  for (int s = 0; s < n; ++s) { const float m = pml[(base + (size_t)s * ROWS) * 2], l = pml[(base + (size_t)s * ROWS) * 2 + 1];
    const float w = __builtin_amdgcn_exp2f(m - M);
    const f32x4 a = *reinterpret_cast<const f32x4*>(po + (base + (size_t)s * ROWS) * D + d);
    L += w * l; o[0] += w * a[0]; o[1] += w * a[1]; o[2] += w * a[2]; o[3] += w * a[3]; }
  const float inv = 1.f / L;
  u32x2 w2 = {cvtpk(o[0] * inv, o[1] * inv), cvtpk(o[2] * inv, o[3] * inv)};
  *reinterpret_cast<u32x2*>(O + (size_t)row * D + d) = w2;
}

extern "C" void kernel_launch(void* const* d_in, const int* in_sizes, int n_in, void* d_out, int out_size, void* d_ws, size_t ws_size, hipStream_t stream) {
  const bf16* q = (const bf16*)d_in[0]; const bf16* k = (const bf16*)d_in[1]; const bf16* v = (const bf16*)d_in[2];
  const int* bt = (const int*)d_in[3]; const int* lens = (const int*)d_in[4];
  float* pml = (float*)((char*)d_ws + WS_PML); float* po = (float*)((char*)d_ws + WS_PO);
  hipLaunchKernelGGL(attn_split, dim3(256), dim3(512), 0, stream, q, k, v, bt, lens, pml, po);
  hipLaunchKernelGGL(attn_combine, dim3(B * HQ * SQ / 8), dim3(256), 0, stream, lens, (const float*)pml, (const float*)po, (bf16*)d_out);
}
```

```cpp
#include <hip/hip_runtime.h>
#include <hip/hip_bf16.h>
#define MIRROR 1
#define FIX_WGS 6u
#define FIX_TILES 3u
#define KVH(g) ((0x67234501u >> (4u * ((g) & 7u))) & 3u)

constexpr int NB = 8, HQ = 32, HKV = 4, SQ = 32, D = 128, PAGE = 16, NBPS = 512, SKV = NBPS * PAGE;
constexpr unsigned PART_STRIDE = 65536u + 2048u;
constexpr unsigned E_OFF = 256u * PART_STRIDE, WS_BYTES = E_OFF + 65536u;
constexpr unsigned LDS_FLAG = 147456u, LDS_BYTES = 147456u + 64u;
constexpr float QSCALE = 0.12751743082f;

typedef unsigned u32x16 __attribute__((ext_vector_type(16)));
typedef unsigned u32x8 __attribute__((ext_vector_type(8)));
typedef unsigned u32x4 __attribute__((ext_vector_type(4)));
typedef unsigned u32x2 __attribute__((ext_vector_type(2)));
typedef float f32x16 __attribute__((ext_vector_type(16)));
typedef float f32x4 __attribute__((ext_vector_type(4)));
typedef float f32x2 __attribute__((ext_vector_type(2)));
typedef __bf16 bf16x8 __attribute__((ext_vector_type(8)));
typedef __bf16 bf16x2 __attribute__((ext_vector_type(2)));
typedef __attribute__((address_space(3))) unsigned lds_u32;
typedef __attribute__((address_space(3))) u32x4 lds_u32x4;
#define DI __device__ __forceinline__
constexpr int SC1 = 16;

DI unsigned rfl(unsigned x) { return __builtin_amdgcn_readfirstlane(x); }
DI unsigned cvtpk(float lo, float hi) { f32x2 v = {lo, hi}; bf16x2 b = __builtin_convertvector(v, bf16x2); return __builtin_bit_cast(unsigned, b); }
DI float bflo(unsigned x) { return __uint_as_float(x << 16); }
DI float bfhi(unsigned x) { return __uint_as_float(x & 0xffff0000u); }

DI void store_unit(const float (&o)[8], float L, unsigned u, unsigned lane, unsigned short* __restrict__ og) {
  const float inv = 1.f / L;
  unsigned short* p = og + ((u >> 2) * 16u + (lane & 15u)) * (unsigned)D + (u & 3u) * 32u + (lane >> 4) * 4u;
  u32x2 a = {cvtpk(o[0] * inv, o[1] * inv), cvtpk(o[2] * inv, o[3] * inv)}, b = {cvtpk(o[4] * inv, o[5] * inv), cvtpk(o[6] * inv, o[7] * inv)};
  *(u32x2*)p = a; *(u32x2*)(p + 16) = b;
}

template <class T> DI T* mkptr(unsigned lo, unsigned hi) { return (T*)(__attribute__((address_space(1))) T*)(((unsigned long long)hi << 32) | lo); }
__global__ __launch_bounds__(256, 1) void attn_main(const int* bt_, unsigned long long btd, const unsigned short* k_, unsigned long long kd,
                                                     const unsigned short* v_, unsigned long long vd, const unsigned short* q_, unsigned long long qd,
                                                     char* ws_, unsigned long long wsd, const int* lens_, unsigned short* out_) {
  unsigned tid = threadIdx.x, g = blockIdx.x;
  const unsigned long long ka = (unsigned long long)__builtin_amdgcn_kernarg_segment_ptr();
  f32x4 mg = {-__builtin_inff(), -__builtin_inff(), -__builtin_inff(), -__builtin_inff()};
  u32x8 so, kb; u32x16 kp;
  for (unsigned pass = 0;; ++pass) {
    asm volatile(
  "s_load_dwordx16 s[40:55], s[8:9], 0\n"
  "s_load_dwordx8 s[56:63], s[8:9], 64\n"
  "s_getpc_b64 s[30:31]\n"
  "Lcw%=:\n"
  "v_lshlrev_b32 v62, 7, v1\n"
  "v_min_u32 v62, Lend%=-Lcw%=-4, v62\n"
  "global_load_dword a198, v62, s[30:31]\n"
  "v_readfirstlane_b32 s92, v1\n"
  "v_and_b32 v24, 63, v1\n"
  "s_lshl_b32 s20, s10, 2\n"
  "s_lshr_b32 s95, 0x67234501, s20\n"
  "s_bfe_u32 s20, s10, 0x20003\n"
  "s_bfe_u32 s98, s95, 0x10002\n"
  "s_and_b32 s95, s95, 3\n"
  "s_lshl1_add_u32 s20, s20, s98\n"
  "s_lshr_b32 s21, s10, 5\n"
  "v_and_b32 v60, 14, v24\n"
  "s_lshr_b32 s92, s92, 6\n"
  "s_mul_i32 s23, s21, 48\n"
  "v_lshlrev_b32 v60, 1, v60\n"
  "s_lshl2_add_u32 s23, s92, s23\n"
  "s_lshl_b32 s98, s20, 11\n"
  "s_add_u32 s23, s23, s98\n"
  "v_lshrrev_b32 v25, 4, v24\n"
  "v_and_b32 v32, 15, v24\n"
  "v_xor_b32 v33, v32, v25\n"
  "v_lshlrev_b32 v33, 4, v33\n"
  "v_lshl_or_b32 v44, v25, 8, v33\n"
  "v_xor_b32 v45, 64, v44\n"
  "v_xor_b32 v46, 128, v44\n"
  "v_xor_b32 v47, 192, v44\n"
  "s_lshl2_add_u32 s27, s95, s92\n"
  "s_lshl_b32 s27, s27, 14\n"
  "s_lshl_b32 s97, s20, 18\n"
  "s_add_u32 s97, s97, s27\n"
  "s_and_b32 s96, s92, 1\n"
  "s_lshr_b32 s98, s92, 1\n"
  "s_lshl2_add_u32 s96, s98, s96\n"
  "s_add_u32 s96, s96, 2\n"
  "s_lshl_b32 s96, s96, 14\n"
  "s_lshl_b32 s91, s92, 12\n"
  "s_lshl_b32 s99, s95, 8\n"
  "v_lshl_or_b32 v8, v32, 8, v33\n"
  "v_or_b32 v33, s99, v33\n"
  "v_lshl_or_b32 v14, v25, 12, v33\n"
  "v_xor_b32 v15, 64, v14\n"
  "v_xor_b32 v16, 128, v14\n"
  "v_xor_b32 v17, 192, v14\n"
  "v_bfe_u32 v33, v24, 2, 2\n"
  "v_and_b32 v34, 3, v24\n"
  "v_lshl_or_b32 v35, v34, 4, s99\n"
  "v_lshl_or_b32 v35, v25, 6, v35\n"
  "v_lshl_or_b32 v18, v33, 12, v35\n"
  "v_xor_b32 v19, 32, v18\n"
  "s_lshl_b32 s28, s95, 10\n"
  "s_add_u32 s28, s28, 17301504\n"
  "s_cmp_ge_u32 s21, 6\n"
  "s_cselect_b32 s22, -1, 0\n"
  "v_and_b32 v36, 1, v24\n"
  "v_lshl_or_b32 v36, v25, 1, v36\n"
  "v_lshlrev_b32 v37, 2, v36\n"
  "s_mov_b32 s98, 0x07421100\n"
  "v_bfe_u32 v97, s98, v37, 4\n"
  "v_cmp_eq_u32 vcc, 7, v36\n"
  "v_bfe_u32 v98, 5, v36, 1\n"
  "v_lshrrev_b32 v99, 2, v60\n"
  "v_cndmask_b32 v97, v97, 21, vcc\n"
  "v_mul_u32_u24 v99, 6, v99\n"
  "v_lshlrev_b32 v97, 6, v97\n"
  "v_add_u32 v97, 87, v97\n"
  "s_mul_i32 s24, s20, 2\n"
  "s_add_u32 s24, s24, s21\n"
  "s_cmp_lg_u32 s22, 0\n"
  "s_cselect_b32 s98, s24, s21\n"
  "s_cselect_b64 vcc, -1, 0\n"
  "v_mov_b32 v100, s98\n"
  "s_sub_u32 s24, s24, 6\n"
  "v_and_b32 v101, s22, v98\n"
  "v_cndmask_b32 v102, 3, v98, vcc\n"
  "v_mul_u32_u24 v36, 6, v98\n"
  "s_cmp_ge_u32 s21, 6\n"
  "s_waitcnt lgkmcnt(0)\n"
  "global_load_dword v96, v60, s[60:61]\n"
  "s_cbranch_scc1 Ldyn%=\n"
  "s_buffer_load_dword s80, s[40:43], s23 offset:0\n"
  "s_buffer_load_dword s81, s[40:43], s23 offset:16\n"
  "s_buffer_load_dword s82, s[40:43], s23 offset:32\n"
  "s_mov_b32 m0, s96\n"
  "s_nop 0\n"
  "buffer_load_dwordx4 v44, s[52:55], s97 offen offset:0 lds\n"
  "v_cndmask_b32 v103, 15, v36, vcc\n"
  "s_mul_i32 s98, s21, 3\n"
  "v_add_u32 v104, s98, v24\n"
  "s_andn2_b32 s88, 7, s22\n"
  "s_mov_b32 s89, 0\n"
  "s_lshl3_add_u32 s19, s20, s21\n"
  "s_lshl_b32 s20, s20, 1\n"
  "s_lshl_b32 s25, 1, s20\n"
  "buffer_load_dwordx4 v45, s[52:55], s97 offen offset:1024 lds\n"
  "s_andn2_b32 s25, s25, s22\n"
  "s_and_b32 s26, s22, 0x5555\n"
  "s_mov_b32 s86, 0xc000c000\n"
  "s_mov_b32 s87, 0xc000c000\n"
  "s_add_u32 s94, s24, 48\n"
  "s_mov_b32 s12, 0\n"
  "v_xor_b32 v9, 64, v8\n"
  "v_xor_b32 v10, 128, v8\n"
  "buffer_load_dwordx4 v46, s[52:55], s97 offen offset:2048 lds\n"
  "v_xor_b32 v11, 192, v8\n"
  "v_lshlrev_b32 v35, 3, v34\n"
  "v_lshl_or_b32 v35, v33, 6, v35\n"
  "v_lshl_or_b32 v35, v25, 10, v35\n"
  "v_bfe_u32 v34, v24, 4, 1\n"
  "v_lshl_or_b32 v35, v34, 5, v35\n"
  "v_or_b32 v12, 0x10000, v35\n"
  "v_xor_b32 v13, 32, v12\n"
  "buffer_load_dwordx4 v47, s[52:55], s97 offen offset:3072 lds\n"
  "v_or_b32 v40, s96, v8\n"
  "v_or_b32 v41, s96, v9\n"
  "v_or_b32 v42, s96, v10\n"
  "v_or_b32 v43, s96, v11\n"
  "s_lshl_b32 s69, s92, 14\n"
  "v_lshl_or_b32 v22, v24, 4, s69\n"
  "v_lshlrev_b32 v23, 3, v1\n"
  "v_or_b32 v23, 0x10000, v23\n"
  "s_add_u32 m0, s96, 4096\n"
  "s_add_u32 s29, s97, 4096\n"
  "buffer_load_dwordx4 v44, s[52:55], s29 offen offset:0 lds\n"
  "s_mov_b32 s65, 3\n"
  "s_movk_i32 s69, 0x4000\n"
  "s_mov_b32 s75, 0xffff0000\n"
  "v_mov_b64 v[128:129], 0\n"
  "v_mov_b64 v[130:131], 0\n"
  "v_mov_b64 v[132:133], 0\n"
  "v_mov_b64 v[134:135], 0\n"
  "v_mov_b64 v[136:137], 0\n"
  "buffer_load_dwordx4 v45, s[52:55], s29 offen offset:1024 lds\n"
  "v_mov_b64 v[138:139], 0\n"
  "v_mov_b64 v[140:141], 0\n"
  "v_mov_b64 v[142:143], 0\n"
  "v_mov_b64 v[144:145], 0\n"
  "v_mov_b64 v[146:147], 0\n"
  "v_mov_b64 v[148:149], 0\n"
  "v_mov_b64 v[150:151], 0\n"
  "v_mov_b64 v[152:153], 0\n"
  "buffer_load_dwordx4 v46, s[52:55], s29 offen offset:2048 lds\n"
  "v_mov_b64 v[154:155], 0\n"
  "v_mov_b64 v[156:157], 0\n"
  "v_mov_b64 v[158:159], 0\n"
  "v_mov_b64 v[160:161], 0\n"
  "v_mov_b64 v[162:163], 0\n"
  "v_mov_b64 v[164:165], 0\n"
  "v_mov_b64 v[166:167], 0\n"
  "v_mov_b64 v[168:169], 0\n"
  "buffer_load_dwordx4 v47, s[52:55], s29 offen offset:3072 lds\n"
  "v_mov_b64 v[170:171], 0\n"
  "v_mov_b64 v[172:173], 0\n"
  "v_mov_b64 v[174:175], 0\n"
  "v_mov_b64 v[176:177], 0\n"
  "v_mov_b64 v[178:179], 0\n"
  "v_mov_b64 v[180:181], 0\n"
  "v_mov_b64 v[182:183], 0\n"
  "v_mov_b64 v[184:185], 0\n"
  "s_add_u32 m0, s96, 8192\n"
  "s_add_u32 s29, s97, 8192\n"
  "buffer_load_dwordx4 v44, s[52:55], s29 offen offset:0 lds\n"
  "v_mov_b64 v[186:187], 0\n"
  "v_mov_b64 v[188:189], 0\n"
  "v_mov_b64 v[190:191], 0\n"
  "v_mov_b64 v[192:193], 0\n"
  "v_mov_b64 v[194:195], 0\n"
  "v_mov_b64 v[196:197], 0\n"
  "v_mov_b64 v[198:199], 0\n"
  "v_mov_b64 v[200:201], 0\n"
  "buffer_load_dwordx4 v45, s[52:55], s29 offen offset:1024 lds\n"
  "v_mov_b64 v[202:203], 0\n"
  "v_mov_b64 v[204:205], 0\n"
  "v_mov_b64 v[206:207], 0\n"
  "v_mov_b64 v[208:209], 0\n"
  "v_mov_b64 v[210:211], 0\n"
  "v_mov_b64 v[212:213], 0\n"
  "v_mov_b64 v[214:215], 0\n"
  "v_mov_b64 v[216:217], 0\n"
  "buffer_load_dwordx4 v46, s[52:55], s29 offen offset:2048 lds\n"
  "v_mov_b64 v[218:219], 0\n"
  "v_mov_b64 v[220:221], 0\n"
  "v_mov_b64 v[222:223], 0\n"
  "v_mov_b64 v[224:225], 0\n"
  "v_mov_b64 v[226:227], 0\n"
  "v_mov_b64 v[228:229], 0\n"
  "v_mov_b64 v[230:231], 0\n"
  "v_mov_b64 v[232:233], 0\n"
  "buffer_load_dwordx4 v47, s[52:55], s29 offen offset:3072 lds\n"
  "v_mov_b64 v[234:235], 0\n"
  "v_mov_b64 v[236:237], 0\n"
  "v_mov_b64 v[238:239], 0\n"
  "v_mov_b64 v[240:241], 0\n"
  "v_mov_b64 v[242:243], 0\n"
  "v_mov_b64 v[244:245], 0\n"
  "v_mov_b64 v[246:247], 0\n"
  "v_mov_b64 v[248:249], 0\n"
  "buffer_load_dwordx2 v[26:27], off, s[56:59], s28 sc1\n"
  "s_waitcnt vmcnt(13) lgkmcnt(0)\n"
  "s_lshl_b32 s80, s80, 14\n"
  "s_lshl_b32 s81, s81, 14\n"
  "s_lshl_b32 s82, s82, 14\n"
  "s_mov_b32 s67, s82\n"
  "s_add_u32 m0, s91, 0\n"
  "s_nop 0\n"
  "buffer_load_dwordx4 v14, s[44:47], s80 offen offset:0 nt lds\n"
  "v_add_u32 v105, -1, v96\n"
  "v_min_u32 v105, 0x1fff, v105\n"
  "v_lshrrev_b32 v106, 6, v105\n"
  "v_add_u32 v123, 1, v106\n"
  "s_nop 0\n"
  "v_add_u32_dpp v107, v106, v106 row_ror:2 row_mask:0xf bank_mask:0xf\n"
  "v_add_u32 v120, 3, v106\n"
  "v_mul_u32_u24 v120, 171, v120\n"
  "v_add_u32_dpp v107, v107, v107 row_ror:4 row_mask:0xf bank_mask:0xf\n"
  "v_lshrrev_b32 v120, 9, v120\n"
  "buffer_load_dwordx4 v15, s[44:47], s80 offen offset:1024 nt lds\n"
  "s_nop 0\n"
  "v_add_u32_dpp v107, v107, v107 row_ror:8 row_mask:0xf bank_mask:0xf\n"
  "v_add_u32 v108, v107, v97\n"
  "v_lshrrev_b32 v108, 6, v108\n"
  "v_max_u32 v108, 4, v108\n"
  "v_sub_u32 v109, v108, v98\n"
  "v_cvt_f32_u32 v109, v109\n"
  "v_mad_i32_i24 v110, v108, -6, v123\n"
  "v_rcp_f32 v109, v109\n"
  "v_cvt_f32_i32 v110, v110\n"
  "buffer_load_dwordx4 v16, s[44:47], s80 offen offset:2048 nt lds\n"
  "v_add_f32 v110, -0.5, v110\n"
  "v_fma_f32 v110, v110, v109, 1.0\n"
  "v_cvt_u32_f32 v109, v110\n"
  "v_add_u32 v113, 6, v109\n"
  "v_mad_u32_u24 v114, v109, v98, v123\n"
  "v_add_u32_dpp v112, v109, v109 row_shr:2 row_mask:0xf bank_mask:0xf bound_ctrl:1\n"
  "v_min_u32 v120, v120, v113\n"
  "v_cvt_f32_u32 v113, v113\n"
  "v_add_u32_dpp v112, v112, v112 row_shr:4 row_mask:0xf bank_mask:0xf bound_ctrl:1\n"
  "v_rcp_f32 v113, v113\n"
  "buffer_load_dwordx4 v17, s[44:47], s80 offen offset:3072 nt lds\n"
  "v_max_u32 v114, 18, v114\n"
  "v_add_u32_dpp v112, v112, v112 row_shr:8 row_mask:0xf bank_mask:0xf bound_ctrl:1\n"
  "v_fma_f32 v111, -0.5, v113, 1.0\n"
  "v_cmp_ge_u32 vcc, 16, v112\n"
  "v_cmp_lt_u32 s[72:73], s24, v112\n"
  "v_sub_u32 v115, v112, v109\n"
  "s_and_b64 vcc, vcc, s[86:87]\n"
  "v_and_b32 v116, s22, v115\n"
  "s_ff1_i32_b64 s98, vcc\n"
  "v_sub_u32 v116, v100, v116\n"
  "s_add_u32 m0, s96, 12288\n"
  "s_add_u32 s29, s97, 12288\n"
  "buffer_load_dwordx4 v44, s[52:55], s29 offen offset:0 lds\n"
  "s_sub_u32 s98, s98, 14\n"
  "v_mul_i32_i24 v117, v116, v114\n"
  "s_lshr_b64 s[72:73], s[72:73], s98\n"
  "v_add_u32 v118, v117, v114\n"
  "s_add_u32 s68, s20, s98\n"
  "v_cvt_f32_i32 v117, v117\n"
  "s_and_b32 s99, s72, s26\n"
  "v_cvt_f32_i32 v118, v118\n"
  "s_or_b32 s99, s99, s25\n"
  "v_add_u32 v121, v99, v115\n"
  "s_cbranch_scc0 Lidle%=\n"
  "buffer_load_dwordx4 v45, s[52:55], s29 offen offset:1024 lds\n"
  "s_ff1_i32_b32 s13, s99\n"
  "v_fma_f32 v117, v117, v113, v111\n"
  "s_add_u32 s98, s98, s13\n"
  "v_fma_f32 v118, v118, v113, v111\n"
  "v_cvt_u32_f32 v117, v117\n"
  "v_cvt_u32_f32 v118, v118\n"
  "v_mul_i32_i24 v115, v102, v116\n"
  "v_sub_u32 v118, v118, v117\n"
  "v_sub_u32 v117, v117, v115\n"
  "v_add_u32 v117, v117, v103\n"
  "buffer_load_dwordx4 v46, s[52:55], s29 offen offset:2048 lds\n"
  "v_mad_i32_i24 v119, v116, -3, v123\n"
  "v_readlane_b32 s14, v117, s98\n"
  "v_sub_u32 v118, v118, v101\n"
  "v_min_i32 v119, v119, v118\n"
  "v_add_u32 v122, v121, v116\n"
  "v_add_u32 v115, s14, v24\n"
  "v_readlane_b32 s64, v119, s98\n"
  "v_cndmask_b32 v115, v115, v104, s[88:89]\n"
  "v_readlane_b32 s94, v122, s98\n"
  "v_min_u32 v107, 127, v115\n"
  "buffer_load_dwordx4 v47, s[52:55], s29 offen offset:3072 lds\n"
  "s_lshl_b32 s99, s13, 8\n"
  "v_lshlrev_b32 v107, 4, v107\n"
  "s_add_u32 s99, s99, s92\n"
  "s_lshl_b32 s66, s99, 2\n"
  "s_cmp_lt_i32 s64, 1\n"
  "s_cbranch_scc1 Lidle%=\n"
  "buffer_load_dword v20, v107, s[40:43], s66 offen\n"
  "v_mov_b64 v[250:251], 0\n"
  "v_mov_b64 v[252:253], 0\n"
  "v_mov_b64 v[254:255], 0\n"
  "s_add_u32 m0, s91, 65536\n"
  "s_nop 0\n"
  "buffer_load_dwordx4 v18, s[48:51], s80 offen offset:0 nt lds\n"
  "v_mov_b32 v30, 0x3f803f80\n"
  "v_accvgpr_write_b32 a192, v30\n"
  "v_accvgpr_write_b32 a193, v30\n"
  "v_accvgpr_write_b32 a194, v30\n"
  "v_accvgpr_write_b32 a195, v30\n"
  "buffer_load_dwordx4 v19, s[48:51], s80 offen offset:1024 nt lds\n"
  "buffer_load_dwordx4 v18, s[48:51], s80 offen offset:2048 nt lds\n"
  "buffer_load_dwordx4 v19, s[48:51], s80 offen offset:3072 nt lds\n"
  "s_branch Ljoin%=\n"
  "Ldyn%=:\n"
  "v_cndmask_b32 v103, 15, v36, vcc\n"
  "s_mul_i32 s98, s21, 3\n"
  "v_add_u32 v104, s98, v24\n"
  "s_andn2_b32 s88, 7, s22\n"
  "s_mov_b32 s89, 0\n"
  "s_lshl3_add_u32 s19, s20, s21\n"
  "s_lshl_b32 s20, s20, 1\n"
  "s_lshl_b32 s25, 1, s20\n"
  "s_andn2_b32 s25, s25, s22\n"
  "s_and_b32 s26, s22, 0x5555\n"
  "s_mov_b32 s86, 0xc000c000\n"
  "s_mov_b32 s87, 0xc000c000\n"
  "s_add_u32 s94, s24, 48\n"
  "s_mov_b32 s12, 0\n"
  "buffer_load_dwordx2 v[26:27], off, s[56:59], s28 sc1\n"
  "s_lshl_b32 s30, s95, 1\n"
  "s_or_b32 s30, s30, s95\n"
  "s_and_b32 s30, s30, 5\n"
  "s_lshl2_add_u32 s36, s24, s92\n"
  "s_lshl_b32 s30, s30, 6\n"
  "s_and_b32 s31, s24, 1\n"
  "s_add_u32 s30, s30, s36\n"
  "s_lshl_b32 s31, s31, 7\n"
  "s_add_u32 s30, s30, s31\n"
  "s_lshl_b32 s30, s30, 6\n"
  "s_add_u32 s30, s30, 17334272\n"
  "s_xor_b32 s31, s30, 0x2000\n"
  "s_lshl3_add_u32 s36, s36, s28\n"
  "v_lshlrev_b32 v61, 4, v24\n"
  "v_xor_b32 v9, 64, v8\n"
  "v_xor_b32 v10, 128, v8\n"
  "v_xor_b32 v11, 192, v8\n"
  "v_lshlrev_b32 v35, 3, v34\n"
  "v_lshl_or_b32 v35, v33, 6, v35\n"
  "v_lshl_or_b32 v35, v25, 10, v35\n"
  "v_bfe_u32 v34, v24, 4, 1\n"
  "v_lshl_or_b32 v35, v34, 5, v35\n"
  "v_or_b32 v12, 0x10000, v35\n"
  "v_xor_b32 v13, 32, v12\n"
  "v_or_b32 v40, s96, v8\n"
  "v_or_b32 v41, s96, v9\n"
  "v_or_b32 v42, s96, v10\n"
  "v_or_b32 v43, s96, v11\n"
  "s_lshl_b32 s69, s92, 14\n"
  "v_lshl_or_b32 v22, v24, 4, s69\n"
  "v_lshlrev_b32 v23, 3, v1\n"
  "v_or_b32 v23, 0x10000, v23\n"
  "s_mov_b32 s65, 3\n"
  "s_movk_i32 s69, 0x4000\n"
  "s_mov_b32 s75, 0xffff0000\n"
  "v_mov_b64 v[128:129], 0\n"
  "v_mov_b64 v[130:131], 0\n"
  "v_mov_b64 v[132:133], 0\n"
  "v_mov_b64 v[134:135], 0\n"
  "v_mov_b64 v[136:137], 0\n"
  "v_mov_b64 v[138:139], 0\n"
  "v_mov_b64 v[140:141], 0\n"
  "v_mov_b64 v[142:143], 0\n"
  "v_mov_b64 v[144:145], 0\n"
  "v_mov_b64 v[146:147], 0\n"
  "v_mov_b64 v[148:149], 0\n"
  "v_mov_b64 v[150:151], 0\n"
  "v_mov_b64 v[152:153], 0\n"
  "v_mov_b64 v[154:155], 0\n"
  "v_mov_b64 v[156:157], 0\n"
  "v_mov_b64 v[158:159], 0\n"
  "v_mov_b64 v[160:161], 0\n"
  "v_mov_b64 v[162:163], 0\n"
  "v_mov_b64 v[164:165], 0\n"
  "v_mov_b64 v[166:167], 0\n"
  "v_mov_b64 v[168:169], 0\n"
  "v_mov_b64 v[170:171], 0\n"
  "v_mov_b64 v[172:173], 0\n"
  "v_mov_b64 v[174:175], 0\n"
  "v_mov_b64 v[176:177], 0\n"
  "v_mov_b64 v[178:179], 0\n"
  "v_mov_b64 v[180:181], 0\n"
  "v_mov_b64 v[182:183], 0\n"
  "v_mov_b64 v[184:185], 0\n"
  "v_mov_b64 v[186:187], 0\n"
  "v_mov_b64 v[188:189], 0\n"
  "v_mov_b64 v[190:191], 0\n"
  "v_mov_b64 v[192:193], 0\n"
  "v_mov_b64 v[194:195], 0\n"
  "v_mov_b64 v[196:197], 0\n"
  "v_mov_b64 v[198:199], 0\n"
  "v_mov_b64 v[200:201], 0\n"
  "v_mov_b64 v[202:203], 0\n"
  "v_mov_b64 v[204:205], 0\n"
  "s_waitcnt vmcnt(1)\n"
  "v_add_u32 v105, -1, v96\n"
  "v_min_u32 v105, 0x1fff, v105\n"
  "v_lshrrev_b32 v106, 6, v105\n"
  "v_add_u32 v123, 1, v106\n"
  "s_nop 0\n"
  "v_add_u32_dpp v107, v106, v106 row_ror:2 row_mask:0xf bank_mask:0xf\n"
  "v_add_u32 v120, 3, v106\n"
  "v_mul_u32_u24 v120, 171, v120\n"
  "v_add_u32_dpp v107, v107, v107 row_ror:4 row_mask:0xf bank_mask:0xf\n"
  "v_lshrrev_b32 v120, 9, v120\n"
  "s_nop 0\n"
  "v_add_u32_dpp v107, v107, v107 row_ror:8 row_mask:0xf bank_mask:0xf\n"
  "v_add_u32 v108, v107, v97\n"
  "v_lshrrev_b32 v108, 6, v108\n"
  "v_max_u32 v108, 4, v108\n"
  "v_sub_u32 v109, v108, v98\n"
  "v_cvt_f32_u32 v109, v109\n"
  "v_mad_i32_i24 v110, v108, -6, v123\n"
  "v_rcp_f32 v109, v109\n"
  "v_cvt_f32_i32 v110, v110\n"
  "v_add_f32 v110, -0.5, v110\n"
  "v_fma_f32 v110, v110, v109, 1.0\n"
  "v_cvt_u32_f32 v109, v110\n"
  "v_add_u32 v113, 6, v109\n"
  "v_mad_u32_u24 v114, v109, v98, v123\n"
  "v_add_u32_dpp v112, v109, v109 row_shr:2 row_mask:0xf bank_mask:0xf bound_ctrl:1\n"
  "v_min_u32 v120, v120, v113\n"
  "v_cvt_f32_u32 v113, v113\n"
  "v_add_u32_dpp v112, v112, v112 row_shr:4 row_mask:0xf bank_mask:0xf bound_ctrl:1\n"
  "v_rcp_f32 v113, v113\n"
  "v_max_u32 v114, 18, v114\n"
  "v_add_u32_dpp v112, v112, v112 row_shr:8 row_mask:0xf bank_mask:0xf bound_ctrl:1\n"
  "v_fma_f32 v111, -0.5, v113, 1.0\n"
  "v_cmp_ge_u32 vcc, 16, v112\n"
  "v_cmp_lt_u32 s[72:73], s24, v112\n"
  "v_sub_u32 v115, v112, v109\n"
  "s_and_b64 vcc, vcc, s[86:87]\n"
  "v_and_b32 v116, s22, v115\n"
  "s_ff1_i32_b64 s98, vcc\n"
  "v_sub_u32 v116, v100, v116\n"
  "s_sub_u32 s98, s98, 14\n"
  "v_mul_i32_i24 v117, v116, v114\n"
  "s_lshr_b64 s[72:73], s[72:73], s98\n"
  "v_add_u32 v118, v117, v114\n"
  "s_add_u32 s68, s20, s98\n"
  "v_cvt_f32_i32 v117, v117\n"
  "s_and_b32 s99, s72, s26\n"
  "v_cvt_f32_i32 v118, v118\n"
  "s_or_b32 s99, s99, s25\n"
  "v_add_u32 v121, v99, v115\n"
  "s_cbranch_scc0 Lidle%=\n"
  "s_ff1_i32_b32 s13, s99\n"
  "v_fma_f32 v117, v117, v113, v111\n"
  "s_add_u32 s98, s98, s13\n"
  "v_fma_f32 v118, v118, v113, v111\n"
  "v_cvt_u32_f32 v117, v117\n"
  "v_cvt_u32_f32 v118, v118\n"
  "v_mul_i32_i24 v115, v102, v116\n"
  "v_sub_u32 v118, v118, v117\n"
  "v_sub_u32 v117, v117, v115\n"
  "v_add_u32 v117, v117, v103\n"
  "v_mad_i32_i24 v119, v116, -3, v123\n"
  "v_readlane_b32 s14, v117, s98\n"
  "v_sub_u32 v118, v118, v101\n"
  "s_mov_b64 exec, 15\n"
  "buffer_store_dwordx4 v[44:47], v61, s[56:59], s30 offen sc1\n"
  "buffer_store_dwordx4 v[44:47], v61, s[56:59], s31 offen sc1\n"
  "s_mov_b64 exec, -1\n"
  "v_min_i32 v119, v119, v118\n"
  "v_add_u32 v122, v121, v116\n"
  "v_add_u32 v115, s14, v24\n"
  "v_readlane_b32 s64, v119, s98\n"
  "v_cndmask_b32 v115, v115, v104, s[88:89]\n"
  "v_readlane_b32 s94, v122, s98\n"
  "v_min_u32 v107, 127, v115\n"
  "s_lshl_b32 s99, s13, 8\n"
  "v_lshlrev_b32 v107, 4, v107\n"
  "s_add_u32 s99, s99, s92\n"
  "s_lshl_b32 s66, s99, 2\n"
  "s_cmp_lt_i32 s64, 1\n"
  "s_cbranch_scc1 Lidle%=\n"
  "buffer_load_dword v20, v107, s[40:43], s66 offen\n"
  "s_lshl_b32 s97, s13, 17\n"
  "s_add_u32 s97, s97, s27\n"
  "s_lshl_b32 s30, s24, 31\n"
  "s_mov_b32 m0, s96\n"
  "s_nop 0\n"
  "buffer_load_dwordx4 v44, s[52:55], s97 offen offset:0 lds\n"
  "v_mov_b64 v[206:207], 0\n"
  "v_mov_b64 v[208:209], 0\n"
  "v_mov_b64 v[210:211], 0\n"
  "v_mov_b64 v[212:213], 0\n"
  "v_mov_b64 v[214:215], 0\n"
  "v_mov_b64 v[216:217], 0\n"
  "v_mov_b64 v[218:219], 0\n"
  "v_mov_b64 v[220:221], 0\n"
  "buffer_load_dwordx4 v45, s[52:55], s97 offen offset:1024 lds\n"
  "v_mov_b64 v[222:223], 0\n"
  "v_mov_b64 v[224:225], 0\n"
  "v_mov_b64 v[226:227], 0\n"
  "v_mov_b64 v[228:229], 0\n"
  "v_mov_b64 v[230:231], 0\n"
  "v_mov_b64 v[232:233], 0\n"
  "v_mov_b64 v[234:235], 0\n"
  "v_mov_b64 v[236:237], 0\n"
  "buffer_load_dwordx4 v46, s[52:55], s97 offen offset:2048 lds\n"
  "v_mov_b64 v[238:239], 0\n"
  "v_mov_b64 v[240:241], 0\n"
  "v_mov_b64 v[242:243], 0\n"
  "v_mov_b64 v[244:245], 0\n"
  "v_mov_b64 v[246:247], 0\n"
  "v_mov_b64 v[248:249], 0\n"
  "v_mov_b64 v[250:251], 0\n"
  "v_mov_b64 v[252:253], 0\n"
  "buffer_load_dwordx4 v47, s[52:55], s97 offen offset:3072 lds\n"
  "v_mov_b64 v[254:255], 0\n"
  "v_mov_b32 v30, 0x3f803f80\n"
  "v_accvgpr_write_b32 a192, v30\n"
  "v_accvgpr_write_b32 a193, v30\n"
  "v_accvgpr_write_b32 a194, v30\n"
  "v_accvgpr_write_b32 a195, v30\n"
  "s_waitcnt vmcnt(6)\n"
  "s_getreg_b32 s31, hwreg(HW_REG_IB_STS, 0, 4)\n"
  "s_cmp_eq_u32 s31, 6\n"
  "s_cselect_b32 s31, s30, 0x40000000\n"
  "s_add_u32 m0, s96, 4096\n"
  "s_add_u32 s29, s97, 4096\n"
  "buffer_load_dwordx4 v44, s[52:55], s29 offen offset:0 lds\n"
  "buffer_load_dwordx4 v45, s[52:55], s29 offen offset:1024 lds\n"
  "buffer_load_dwordx4 v46, s[52:55], s29 offen offset:2048 lds\n"
  "buffer_load_dwordx4 v47, s[52:55], s29 offen offset:3072 lds\n"
  "s_add_u32 m0, s96, 8192\n"
  "s_add_u32 s29, s97, 8192\n"
  "buffer_load_dwordx4 v44, s[52:55], s29 offen offset:0 lds\n"
  "buffer_load_dwordx4 v45, s[52:55], s29 offen offset:1024 lds\n"
  "buffer_load_dwordx4 v46, s[52:55], s29 offen offset:2048 lds\n"
  "buffer_load_dwordx4 v47, s[52:55], s29 offen offset:3072 lds\n"
  "s_waitcnt vmcnt(12)\n"
  "v_readlane_b32 s80, v20, 0\n"
  "v_readlane_b32 s81, v20, 1\n"
  "v_readlane_b32 s82, v20, 2\n"
  "s_lshl_b32 s80, s80, 14\n"
  "s_lshl_b32 s81, s81, 14\n"
  "s_lshl_b32 s82, s82, 14\n"
  "s_mov_b32 s67, s82\n"
  "s_add_u32 m0, s91, 0\n"
  "s_nop 0\n"
  "buffer_load_dwordx4 v14, s[44:47], s80 offen offset:0 nt lds\n"
  "buffer_load_dwordx4 v15, s[44:47], s80 offen offset:1024 nt lds\n"
  "buffer_load_dwordx4 v16, s[44:47], s80 offen offset:2048 nt lds\n"
  "buffer_load_dwordx4 v17, s[44:47], s80 offen offset:3072 nt lds\n"
  "s_add_u32 m0, s96, 12288\n"
  "s_add_u32 s29, s97, 12288\n"
  "buffer_load_dwordx4 v44, s[52:55], s29 offen offset:0 lds\n"
  "buffer_load_dwordx4 v45, s[52:55], s29 offen offset:1024 lds\n"
  "buffer_load_dwordx4 v46, s[52:55], s29 offen offset:2048 lds\n"
  "buffer_load_dwordx4 v47, s[52:55], s29 offen offset:3072 lds\n"
  "s_cmp_eq_u32 s11, 0\n"
  "s_cselect_b32 s37, 0, 0xf0000000\n"
  "v_add_u32 v62, 1, v26\n"
  "v_xor_b32 v63, s31, v27\n"
  "v_mov_b32 v61, s37\n"
  "s_mov_b64 exec, 1\n"
  "buffer_store_dwordx2 v[62:63], v61, s[56:59], s36 offen offset:512 sc1\n"
  "s_mov_b64 exec, -1\n"
  "s_add_u32 m0, s91, 65536\n"
  "s_nop 0\n"
  "buffer_load_dwordx4 v18, s[48:51], s80 offen offset:0 nt lds\n"
  "buffer_load_dwordx4 v19, s[48:51], s80 offen offset:1024 nt lds\n"
  "buffer_load_dwordx4 v18, s[48:51], s80 offen offset:2048 nt lds\n"
  "buffer_load_dwordx4 v19, s[48:51], s80 offen offset:3072 nt lds\n"
  "Ljoin%=:\n"
  "s_waitcnt vmcnt(17)\n"
  "ds_read_b128 v[64:67], v40 offset:0\n"
  "ds_read_b128 v[68:71], v41 offset:0\n"
  "ds_read_b128 v[72:75], v42 offset:0\n"
  "ds_read_b128 v[76:79], v43 offset:0\n"
  "ds_read_b128 v[80:83], v40 offset:4096\n"
  "ds_read_b128 v[84:87], v41 offset:4096\n"
  "ds_read_b128 v[88:91], v42 offset:4096\n"
  "ds_read_b128 v[92:95], v43 offset:4096\n"
  "v_readlane_b32 s16, v105, s98\n"
  "v_readlane_b32 s93, v106, s98\n"
  "v_readlane_b32 s74, v96, s98\n"
  "s_sub_u32 s99, s64, 1\n"
  "v_readlane_b32 s17, v120, s68\n"
  "v_readlane_b32 s18, v121, s68\n"
  "v_readlane_b32 s99, v115, s99\n"
  "v_cmp_gt_u32 vcc, s64, v24\n"
  "v_mov_b32 v29, 0xf0000000\n"
  "s_cmp_gt_i32 s74, 0\n"
  "s_cselect_b32 s74, 0x3e0293ee, 0\n"
  "v_cndmask_b32 v29, v29, 0, vcc\n"
  "s_and_b32 s98, s16, 63\n"
  "s_add_u32 s98, s98, 1\n"
  "s_cmp_eq_u32 s99, s93\n"
  "s_cselect_b32 s98, s98, 64\n"
  "v_sub_u32 v21, s98, v25\n"
  "s_add_u32 s16, s16, 1\n"
  "s_add_u32 s15, s14, s64\n"
  "s_lshr_b32 s13, s13, 1\n"
  "s_cmp_eq_u32 s64, 1\n"
  "s_cselect_b32 s93, 1, 0\n"
  "s_add_u32 s64, s64, 3\n"
  "s_min_u32 s20, s64, 5\n"
  "s_sub_u32 s99, s64, 1\n"
  "s_cmp_lt_u32 s98, 64\n"
  "s_cselect_b32 s68, s99, -1\n"
  "s_cselect_b32 s90, 1, 0\n"
  "s_cselect_b32 s93, s93, 0\n"
  "s_add_u32 s66, s91, 32768\n"
  "s_lshl_b32 s99, s95, 6\n"
  "s_add_u32 s99, s99, s94\n"
  "s_mul_i32 s76, s99, 67584\n"
  "s_add_u32 s77, s76, 4096\n"
  "s_add_u32 s78, s76, 8192\n"
  "s_add_u32 s79, s76, 12288\n"
  "s_waitcnt lgkmcnt(6)\n"
  "v_lshlrev_b32 v48, 16, v64\n"
  "v_and_b32 v49, s75, v64\n"
  "v_lshlrev_b32 v50, 16, v65\n"
  "v_and_b32 v51, s75, v65\n"
  "v_lshlrev_b32 v52, 16, v66\n"
  "v_and_b32 v53, s75, v66\n"
  "v_lshlrev_b32 v54, 16, v67\n"
  "v_and_b32 v55, s75, v67\n"
  "v_pk_mul_f32 v[48:49], v[48:49], s[74:75] op_sel_hi:[1,0]\n"
  "v_pk_mul_f32 v[50:51], v[50:51], s[74:75] op_sel_hi:[1,0]\n"
  "v_pk_mul_f32 v[52:53], v[52:53], s[74:75] op_sel_hi:[1,0]\n"
  "v_pk_mul_f32 v[54:55], v[54:55], s[74:75] op_sel_hi:[1,0]\n"
  "v_cvt_pk_bf16_f32 v64, v48, v49\n"
  "v_cvt_pk_bf16_f32 v65, v50, v51\n"
  "v_cvt_pk_bf16_f32 v66, v52, v53\n"
  "v_cvt_pk_bf16_f32 v67, v54, v55\n"
  "v_accvgpr_write_b32 a0, v64\n"
  "v_accvgpr_write_b32 a1, v65\n"
  "v_accvgpr_write_b32 a2, v66\n"
  "v_accvgpr_write_b32 a3, v67\n"
  "s_add_u32 m0, s91, 16384\n"
  "s_nop 0\n"
  "buffer_load_dwordx4 v14, s[44:47], s81 offen offset:0 nt lds\n"
  "v_lshlrev_b32 v56, 16, v68\n"
  "v_and_b32 v57, s75, v68\n"
  "v_lshlrev_b32 v58, 16, v69\n"
  "v_and_b32 v59, s75, v69\n"
  "v_lshlrev_b32 v60, 16, v70\n"
  "v_and_b32 v61, s75, v70\n"
  "v_lshlrev_b32 v62, 16, v71\n"
  "v_and_b32 v63, s75, v71\n"
  "v_pk_mul_f32 v[56:57], v[56:57], s[74:75] op_sel_hi:[1,0]\n"
  "v_pk_mul_f32 v[58:59], v[58:59], s[74:75] op_sel_hi:[1,0]\n"
  "v_pk_mul_f32 v[60:61], v[60:61], s[74:75] op_sel_hi:[1,0]\n"
  "v_pk_mul_f32 v[62:63], v[62:63], s[74:75] op_sel_hi:[1,0]\n"
  "v_cvt_pk_bf16_f32 v68, v56, v57\n"
  "v_cvt_pk_bf16_f32 v69, v58, v59\n"
  "v_cvt_pk_bf16_f32 v70, v60, v61\n"
  "v_cvt_pk_bf16_f32 v71, v62, v63\n"
  "v_accvgpr_write_b32 a4, v68\n"
  "v_accvgpr_write_b32 a5, v69\n"
  "v_accvgpr_write_b32 a6, v70\n"
  "v_accvgpr_write_b32 a7, v71\n"
  "buffer_load_dwordx4 v15, s[44:47], s81 offen offset:1024 nt lds\n"
  "s_waitcnt lgkmcnt(4)\n"
  "v_lshlrev_b32 v48, 16, v72\n"
  "v_and_b32 v49, s75, v72\n"
  "v_lshlrev_b32 v50, 16, v73\n"
  "v_and_b32 v51, s75, v73\n"
  "v_lshlrev_b32 v52, 16, v74\n"
  "v_and_b32 v53, s75, v74\n"
  "v_lshlrev_b32 v54, 16, v75\n"
  "v_and_b32 v55, s75, v75\n"
  "v_pk_mul_f32 v[48:49], v[48:49], s[74:75] op_sel_hi:[1,0]\n"
  "v_pk_mul_f32 v[50:51], v[50:51], s[74:75] op_sel_hi:[1,0]\n"
  "v_pk_mul_f32 v[52:53], v[52:53], s[74:75] op_sel_hi:[1,0]\n"
  "v_pk_mul_f32 v[54:55], v[54:55], s[74:75] op_sel_hi:[1,0]\n"
  "v_cvt_pk_bf16_f32 v72, v48, v49\n"
  "v_cvt_pk_bf16_f32 v73, v50, v51\n"
  "v_cvt_pk_bf16_f32 v74, v52, v53\n"
  "v_cvt_pk_bf16_f32 v75, v54, v55\n"
  "v_accvgpr_write_b32 a8, v72\n"
  "v_accvgpr_write_b32 a9, v73\n"
  "v_accvgpr_write_b32 a10, v74\n"
  "v_accvgpr_write_b32 a11, v75\n"
  "buffer_load_dwordx4 v16, s[44:47], s81 offen offset:2048 nt lds\n"
  "v_lshlrev_b32 v56, 16, v76\n"
  "v_and_b32 v57, s75, v76\n"
  "v_lshlrev_b32 v58, 16, v77\n"
  "v_and_b32 v59, s75, v77\n"
  "v_lshlrev_b32 v60, 16, v78\n"
  "v_and_b32 v61, s75, v78\n"
  "v_lshlrev_b32 v62, 16, v79\n"
  "v_and_b32 v63, s75, v79\n"
  "v_pk_mul_f32 v[56:57], v[56:57], s[74:75] op_sel_hi:[1,0]\n"
  "v_pk_mul_f32 v[58:59], v[58:59], s[74:75] op_sel_hi:[1,0]\n"
  "v_pk_mul_f32 v[60:61], v[60:61], s[74:75] op_sel_hi:[1,0]\n"
  "v_pk_mul_f32 v[62:63], v[62:63], s[74:75] op_sel_hi:[1,0]\n"
  "v_cvt_pk_bf16_f32 v76, v56, v57\n"
  "v_cvt_pk_bf16_f32 v77, v58, v59\n"
  "v_cvt_pk_bf16_f32 v78, v60, v61\n"
  "v_cvt_pk_bf16_f32 v79, v62, v63\n"
  "v_accvgpr_write_b32 a12, v76\n"
  "v_accvgpr_write_b32 a13, v77\n"
  "v_accvgpr_write_b32 a14, v78\n"
  "v_accvgpr_write_b32 a15, v79\n"
  "buffer_load_dwordx4 v17, s[44:47], s81 offen offset:3072 nt lds\n"
  "s_waitcnt lgkmcnt(2)\n"
  "v_lshlrev_b32 v48, 16, v80\n"
  "v_and_b32 v49, s75, v80\n"
  "v_lshlrev_b32 v50, 16, v81\n"
  "v_and_b32 v51, s75, v81\n"
  "v_lshlrev_b32 v52, 16, v82\n"
  "v_and_b32 v53, s75, v82\n"
  "v_lshlrev_b32 v54, 16, v83\n"
  "v_and_b32 v55, s75, v83\n"
  "v_pk_mul_f32 v[48:49], v[48:49], s[74:75] op_sel_hi:[1,0]\n"
  "v_pk_mul_f32 v[50:51], v[50:51], s[74:75] op_sel_hi:[1,0]\n"
  "v_pk_mul_f32 v[52:53], v[52:53], s[74:75] op_sel_hi:[1,0]\n"
  "v_pk_mul_f32 v[54:55], v[54:55], s[74:75] op_sel_hi:[1,0]\n"
  "v_cvt_pk_bf16_f32 v80, v48, v49\n"
  "v_cvt_pk_bf16_f32 v81, v50, v51\n"
  "v_cvt_pk_bf16_f32 v82, v52, v53\n"
  "v_cvt_pk_bf16_f32 v83, v54, v55\n"
  "v_accvgpr_write_b32 a16, v80\n"
  "v_accvgpr_write_b32 a17, v81\n"
  "v_accvgpr_write_b32 a18, v82\n"
  "v_accvgpr_write_b32 a19, v83\n"
  "s_add_u32 m0, s91, 81920\n"
  "s_nop 0\n"
  "buffer_load_dwordx4 v18, s[48:51], s81 offen offset:0 nt lds\n"
  "v_lshlrev_b32 v56, 16, v84\n"
  "v_and_b32 v57, s75, v84\n"
  "v_lshlrev_b32 v58, 16, v85\n"
  "v_and_b32 v59, s75, v85\n"
  "v_lshlrev_b32 v60, 16, v86\n"
  "v_and_b32 v61, s75, v86\n"
  "v_lshlrev_b32 v62, 16, v87\n"
  "v_and_b32 v63, s75, v87\n"
  "v_pk_mul_f32 v[56:57], v[56:57], s[74:75] op_sel_hi:[1,0]\n"
  "v_pk_mul_f32 v[58:59], v[58:59], s[74:75] op_sel_hi:[1,0]\n"
  "v_pk_mul_f32 v[60:61], v[60:61], s[74:75] op_sel_hi:[1,0]\n"
  "v_pk_mul_f32 v[62:63], v[62:63], s[74:75] op_sel_hi:[1,0]\n"
  "v_cvt_pk_bf16_f32 v84, v56, v57\n"
  "v_cvt_pk_bf16_f32 v85, v58, v59\n"
  "v_cvt_pk_bf16_f32 v86, v60, v61\n"
  "v_cvt_pk_bf16_f32 v87, v62, v63\n"
  "v_accvgpr_write_b32 a20, v84\n"
  "v_accvgpr_write_b32 a21, v85\n"
  "v_accvgpr_write_b32 a22, v86\n"
  "v_accvgpr_write_b32 a23, v87\n"
  "buffer_load_dwordx4 v19, s[48:51], s81 offen offset:1024 nt lds\n"
  "s_waitcnt lgkmcnt(0)\n"
  "v_lshlrev_b32 v48, 16, v88\n"
  "v_and_b32 v49, s75, v88\n"
  "v_lshlrev_b32 v50, 16, v89\n"
  "v_and_b32 v51, s75, v89\n"
  "v_lshlrev_b32 v52, 16, v90\n"
  "v_and_b32 v53, s75, v90\n"
  "v_lshlrev_b32 v54, 16, v91\n"
  "v_and_b32 v55, s75, v91\n"
  "v_pk_mul_f32 v[48:49], v[48:49], s[74:75] op_sel_hi:[1,0]\n"
  "v_pk_mul_f32 v[50:51], v[50:51], s[74:75] op_sel_hi:[1,0]\n"
  "v_pk_mul_f32 v[52:53], v[52:53], s[74:75] op_sel_hi:[1,0]\n"
  "v_pk_mul_f32 v[54:55], v[54:55], s[74:75] op_sel_hi:[1,0]\n"
  "v_cvt_pk_bf16_f32 v88, v48, v49\n"
  "v_cvt_pk_bf16_f32 v89, v50, v51\n"
  "v_cvt_pk_bf16_f32 v90, v52, v53\n"
  "v_cvt_pk_bf16_f32 v91, v54, v55\n"
  "v_accvgpr_write_b32 a24, v88\n"
  "v_accvgpr_write_b32 a25, v89\n"
  "v_accvgpr_write_b32 a26, v90\n"
  "v_accvgpr_write_b32 a27, v91\n"
  "buffer_load_dwordx4 v18, s[48:51], s81 offen offset:2048 nt lds\n"
  "v_lshlrev_b32 v56, 16, v92\n"
  "v_and_b32 v57, s75, v92\n"
  "v_lshlrev_b32 v58, 16, v93\n"
  "v_and_b32 v59, s75, v93\n"
  "v_lshlrev_b32 v60, 16, v94\n"
  "v_and_b32 v61, s75, v94\n"
  "v_lshlrev_b32 v62, 16, v95\n"
  "v_and_b32 v63, s75, v95\n"
  "v_pk_mul_f32 v[56:57], v[56:57], s[74:75] op_sel_hi:[1,0]\n"
  "v_pk_mul_f32 v[58:59], v[58:59], s[74:75] op_sel_hi:[1,0]\n"
  "v_pk_mul_f32 v[60:61], v[60:61], s[74:75] op_sel_hi:[1,0]\n"
  "v_pk_mul_f32 v[62:63], v[62:63], s[74:75] op_sel_hi:[1,0]\n"
  "v_cvt_pk_bf16_f32 v92, v56, v57\n"
  "v_cvt_pk_bf16_f32 v93, v58, v59\n"
  "v_cvt_pk_bf16_f32 v94, v60, v61\n"
  "v_cvt_pk_bf16_f32 v95, v62, v63\n"
  "v_accvgpr_write_b32 a28, v92\n"
  "v_accvgpr_write_b32 a29, v93\n"
  "v_accvgpr_write_b32 a30, v94\n"
  "v_accvgpr_write_b32 a31, v95\n"
  "buffer_load_dwordx4 v19, s[48:51], s81 offen offset:3072 nt lds\n"
  "s_waitcnt vmcnt(13)\n"
  "ds_read_b128 v[96:99], v40 offset:8192\n"
  "ds_read_b128 v[100:103], v41 offset:8192\n"
  "ds_read_b128 v[104:107], v42 offset:8192\n"
  "ds_read_b128 v[108:111], v43 offset:8192\n"
  "ds_read_b128 v[112:115], v40 offset:12288\n"
  "ds_read_b128 v[116:119], v41 offset:12288\n"
  "ds_read_b128 v[120:123], v42 offset:12288\n"
  "ds_read_b128 v[124:127], v43 offset:12288\n"
  "s_waitcnt lgkmcnt(6)\n"
  "v_lshlrev_b32 v48, 16, v96\n"
  "v_and_b32 v49, s75, v96\n"
  "v_lshlrev_b32 v50, 16, v97\n"
  "v_and_b32 v51, s75, v97\n"
  "v_lshlrev_b32 v52, 16, v98\n"
  "v_and_b32 v53, s75, v98\n"
  "v_lshlrev_b32 v54, 16, v99\n"
  "v_and_b32 v55, s75, v99\n"
  "v_pk_mul_f32 v[48:49], v[48:49], s[74:75] op_sel_hi:[1,0]\n"
  "v_pk_mul_f32 v[50:51], v[50:51], s[74:75] op_sel_hi:[1,0]\n"
  "v_pk_mul_f32 v[52:53], v[52:53], s[74:75] op_sel_hi:[1,0]\n"
  "v_pk_mul_f32 v[54:55], v[54:55], s[74:75] op_sel_hi:[1,0]\n"
  "v_cvt_pk_bf16_f32 v96, v48, v49\n"
  "v_cvt_pk_bf16_f32 v97, v50, v51\n"
  "v_cvt_pk_bf16_f32 v98, v52, v53\n"
  "v_cvt_pk_bf16_f32 v99, v54, v55\n"
  "v_accvgpr_write_b32 a32, v96\n"
  "v_accvgpr_write_b32 a33, v97\n"
  "v_accvgpr_write_b32 a34, v98\n"
  "v_accvgpr_write_b32 a35, v99\n"
  "v_lshlrev_b32 v56, 16, v100\n"
  "v_and_b32 v57, s75, v100\n"
  "v_lshlrev_b32 v58, 16, v101\n"
  "v_and_b32 v59, s75, v101\n"
  "v_lshlrev_b32 v60, 16, v102\n"
  "v_and_b32 v61, s75, v102\n"
  "v_lshlrev_b32 v62, 16, v103\n"
  "v_and_b32 v63, s75, v103\n"
  "v_pk_mul_f32 v[56:57], v[56:57], s[74:75] op_sel_hi:[1,0]\n"
  "v_pk_mul_f32 v[58:59], v[58:59], s[74:75] op_sel_hi:[1,0]\n"
  "v_pk_mul_f32 v[60:61], v[60:61], s[74:75] op_sel_hi:[1,0]\n"
  "v_pk_mul_f32 v[62:63], v[62:63], s[74:75] op_sel_hi:[1,0]\n"
  "v_cvt_pk_bf16_f32 v100, v56, v57\n"
  "v_cvt_pk_bf16_f32 v101, v58, v59\n"
  "v_cvt_pk_bf16_f32 v102, v60, v61\n"
  "v_cvt_pk_bf16_f32 v103, v62, v63\n"
  "v_accvgpr_write_b32 a36, v100\n"
  "v_accvgpr_write_b32 a37, v101\n"
  "v_accvgpr_write_b32 a38, v102\n"
  "v_accvgpr_write_b32 a39, v103\n"
  "s_waitcnt lgkmcnt(4)\n"
  "v_lshlrev_b32 v48, 16, v104\n"
  "v_and_b32 v49, s75, v104\n"
  "v_lshlrev_b32 v50, 16, v105\n"
  "v_and_b32 v51, s75, v105\n"
  "v_lshlrev_b32 v52, 16, v106\n"
  "v_and_b32 v53, s75, v106\n"
  "v_lshlrev_b32 v54, 16, v107\n"
  "v_and_b32 v55, s75, v107\n"
  "v_pk_mul_f32 v[48:49], v[48:49], s[74:75] op_sel_hi:[1,0]\n"
  "v_pk_mul_f32 v[50:51], v[50:51], s[74:75] op_sel_hi:[1,0]\n"
  "v_pk_mul_f32 v[52:53], v[52:53], s[74:75] op_sel_hi:[1,0]\n"
  "v_pk_mul_f32 v[54:55], v[54:55], s[74:75] op_sel_hi:[1,0]\n"
  "v_cvt_pk_bf16_f32 v104, v48, v49\n"
  "v_cvt_pk_bf16_f32 v105, v50, v51\n"
  "v_cvt_pk_bf16_f32 v106, v52, v53\n"
  "v_cvt_pk_bf16_f32 v107, v54, v55\n"
  "v_accvgpr_write_b32 a40, v104\n"
  "v_accvgpr_write_b32 a41, v105\n"
  "v_accvgpr_write_b32 a42, v106\n"
  "v_accvgpr_write_b32 a43, v107\n"
  "v_lshlrev_b32 v56, 16, v108\n"
  "v_and_b32 v57, s75, v108\n"
  "v_lshlrev_b32 v58, 16, v109\n"
  "v_and_b32 v59, s75, v109\n"
  "v_lshlrev_b32 v60, 16, v110\n"
  "v_and_b32 v61, s75, v110\n"
  "v_lshlrev_b32 v62, 16, v111\n"
  "v_and_b32 v63, s75, v111\n"
  "v_pk_mul_f32 v[56:57], v[56:57], s[74:75] op_sel_hi:[1,0]\n"
  "v_pk_mul_f32 v[58:59], v[58:59], s[74:75] op_sel_hi:[1,0]\n"
  "v_pk_mul_f32 v[60:61], v[60:61], s[74:75] op_sel_hi:[1,0]\n"
  "v_pk_mul_f32 v[62:63], v[62:63], s[74:75] op_sel_hi:[1,0]\n"
  "v_cvt_pk_bf16_f32 v108, v56, v57\n"
  "v_cvt_pk_bf16_f32 v109, v58, v59\n"
  "v_cvt_pk_bf16_f32 v110, v60, v61\n"
  "v_cvt_pk_bf16_f32 v111, v62, v63\n"
  "v_accvgpr_write_b32 a44, v108\n"
  "v_accvgpr_write_b32 a45, v109\n"
  "v_accvgpr_write_b32 a46, v110\n"
  "v_accvgpr_write_b32 a47, v111\n"
  "s_waitcnt lgkmcnt(2)\n"
  "v_lshlrev_b32 v48, 16, v112\n"
  "v_and_b32 v49, s75, v112\n"
  "v_lshlrev_b32 v50, 16, v113\n"
  "v_and_b32 v51, s75, v113\n"
  "v_lshlrev_b32 v52, 16, v114\n"
  "v_and_b32 v53, s75, v114\n"
  "v_lshlrev_b32 v54, 16, v115\n"
  "v_and_b32 v55, s75, v115\n"
  "v_pk_mul_f32 v[48:49], v[48:49], s[74:75] op_sel_hi:[1,0]\n"
  "v_pk_mul_f32 v[50:51], v[50:51], s[74:75] op_sel_hi:[1,0]\n"
  "v_pk_mul_f32 v[52:53], v[52:53], s[74:75] op_sel_hi:[1,0]\n"
  "v_pk_mul_f32 v[54:55], v[54:55], s[74:75] op_sel_hi:[1,0]\n"
  "v_cvt_pk_bf16_f32 v112, v48, v49\n"
  "v_cvt_pk_bf16_f32 v113, v50, v51\n"
  "v_cvt_pk_bf16_f32 v114, v52, v53\n"
  "v_cvt_pk_bf16_f32 v115, v54, v55\n"
  "v_accvgpr_write_b32 a48, v112\n"
  "v_accvgpr_write_b32 a49, v113\n"
  "v_accvgpr_write_b32 a50, v114\n"
  "v_accvgpr_write_b32 a51, v115\n"
  "v_lshlrev_b32 v56, 16, v116\n"
  "v_and_b32 v57, s75, v116\n"
  "v_lshlrev_b32 v58, 16, v117\n"
  "v_and_b32 v59, s75, v117\n"
  "v_lshlrev_b32 v60, 16, v118\n"
  "v_and_b32 v61, s75, v118\n"
  "v_lshlrev_b32 v62, 16, v119\n"
  "v_and_b32 v63, s75, v119\n"
  "v_pk_mul_f32 v[56:57], v[56:57], s[74:75] op_sel_hi:[1,0]\n"
  "v_pk_mul_f32 v[58:59], v[58:59], s[74:75] op_sel_hi:[1,0]\n"
  "v_pk_mul_f32 v[60:61], v[60:61], s[74:75] op_sel_hi:[1,0]\n"
  "v_pk_mul_f32 v[62:63], v[62:63], s[74:75] op_sel_hi:[1,0]\n"
  "v_cvt_pk_bf16_f32 v116, v56, v57\n"
  "v_cvt_pk_bf16_f32 v117, v58, v59\n"
  "v_cvt_pk_bf16_f32 v118, v60, v61\n"
  "v_cvt_pk_bf16_f32 v119, v62, v63\n"
  "v_accvgpr_write_b32 a52, v116\n"
  "v_accvgpr_write_b32 a53, v117\n"
  "v_accvgpr_write_b32 a54, v118\n"
  "v_accvgpr_write_b32 a55, v119\n"
  "s_waitcnt lgkmcnt(0)\n"
  "v_lshlrev_b32 v48, 16, v120\n"
  "v_and_b32 v49, s75, v120\n"
  "v_lshlrev_b32 v50, 16, v121\n"
  "v_and_b32 v51, s75, v121\n"
  "v_lshlrev_b32 v52, 16, v122\n"
  "v_and_b32 v53, s75, v122\n"
  "v_lshlrev_b32 v54, 16, v123\n"
  "v_and_b32 v55, s75, v123\n"
  "v_pk_mul_f32 v[48:49], v[48:49], s[74:75] op_sel_hi:[1,0]\n"
  "v_pk_mul_f32 v[50:51], v[50:51], s[74:75] op_sel_hi:[1,0]\n"
  "v_pk_mul_f32 v[52:53], v[52:53], s[74:75] op_sel_hi:[1,0]\n"
  "v_pk_mul_f32 v[54:55], v[54:55], s[74:75] op_sel_hi:[1,0]\n"
  "v_cvt_pk_bf16_f32 v120, v48, v49\n"
  "v_cvt_pk_bf16_f32 v121, v50, v51\n"
  "v_cvt_pk_bf16_f32 v122, v52, v53\n"
  "v_cvt_pk_bf16_f32 v123, v54, v55\n"
  "v_accvgpr_write_b32 a56, v120\n"
  "v_accvgpr_write_b32 a57, v121\n"
  "v_accvgpr_write_b32 a58, v122\n"
  "v_accvgpr_write_b32 a59, v123\n"
  "v_lshlrev_b32 v56, 16, v124\n"
  "v_and_b32 v57, s75, v124\n"
  "v_lshlrev_b32 v58, 16, v125\n"
  "v_and_b32 v59, s75, v125\n"
  "v_lshlrev_b32 v60, 16, v126\n"
  "v_and_b32 v61, s75, v126\n"
  "v_lshlrev_b32 v62, 16, v127\n"
  "v_and_b32 v63, s75, v127\n"
  "v_pk_mul_f32 v[56:57], v[56:57], s[74:75] op_sel_hi:[1,0]\n"
  "v_pk_mul_f32 v[58:59], v[58:59], s[74:75] op_sel_hi:[1,0]\n"
  "v_pk_mul_f32 v[60:61], v[60:61], s[74:75] op_sel_hi:[1,0]\n"
  "v_pk_mul_f32 v[62:63], v[62:63], s[74:75] op_sel_hi:[1,0]\n"
  "v_cvt_pk_bf16_f32 v124, v56, v57\n"
  "v_cvt_pk_bf16_f32 v125, v58, v59\n"
  "v_cvt_pk_bf16_f32 v126, v60, v61\n"
  "v_cvt_pk_bf16_f32 v127, v62, v63\n"
  "v_accvgpr_write_b32 a60, v124\n"
  "v_accvgpr_write_b32 a61, v125\n"
  "v_accvgpr_write_b32 a62, v126\n"
  "v_accvgpr_write_b32 a63, v127\n"
  "s_waitcnt vmcnt(17) lgkmcnt(0)\n"
  "s_barrier\n"
  "Lpeel%=:\n"
  "ds_read_b128 a[64:67], v8 offset:0\n"
  "s_add_u32 m0, s91, 32768\n"
  "v_mov_b64 v[48:49], 0\n"
  "v_mov_b64 v[50:51], 0\n"
  "v_mov_b64 v[52:53], 0\n"
  "v_mov_b64 v[54:55], 0\n"
  "v_mov_b64 v[56:57], 0\n"
  "v_mov_b64 v[58:59], 0\n"
  "v_mov_b64 v[60:61], 0\n"
  "v_mov_b64 v[62:63], 0\n"
  "ds_read_b128 a[68:71], v9 offset:0\n"
  "ds_read_b128 a[72:75], v10 offset:0\n"
  "ds_read_b128 a[76:79], v11 offset:0\n"
  "ds_read_b128 a[80:83], v8 offset:4096\n"
  "ds_read_b128 a[84:87], v9 offset:4096\n"
  "ds_read_b128 a[88:91], v10 offset:4096\n"
  "ds_read_b128 a[92:95], v11 offset:4096\n"
  "ds_read_b128 a[96:99], v8 offset:8192\n"
  "ds_read_b128 a[100:103], v9 offset:8192\n"
  "ds_read_b128 a[104:107], v10 offset:8192\n"
  "ds_read_b128 a[108:111], v11 offset:8192\n"
  "ds_read_b128 a[112:115], v8 offset:12288\n"
  "v_xor_b32 v8, s69, v8\n"
  "ds_read_b128 a[116:119], v9 offset:12288\n"
  "v_xor_b32 v9, s69, v9\n"
  "ds_read_b128 a[120:123], v10 offset:12288\n"
  "v_xor_b32 v10, s69, v10\n"
  "ds_read_b128 a[124:127], v11 offset:12288\n"
  "v_xor_b32 v11, s69, v11\n"
  "buffer_load_dwordx4 v14, s[44:47], s82 offen offset:0 nt lds\n"
  "s_waitcnt lgkmcnt(15)\n"
  "v_mfma_f32_16x16x32_bf16 v[64:67], a[64:67], a[0:3], 0\n"
  "buffer_load_dwordx4 v15, s[44:47], s82 offen offset:1024 nt lds\n"
  "s_waitcnt lgkmcnt(14)\n"
  "v_mfma_f32_16x16x32_bf16 v[64:67], a[68:71], a[4:7], v[64:67]\n"
  "buffer_load_dwordx4 v16, s[44:47], s82 offen offset:2048 nt lds\n"
  "s_waitcnt lgkmcnt(6)\n"
  "v_mfma_f32_16x16x32_bf16 v[64:67], a[72:75], a[8:11], v[64:67]\n"
  "buffer_load_dwordx4 v17, s[44:47], s82 offen offset:3072 nt lds\n"
  "v_mfma_f32_16x16x32_bf16 v[64:67], a[76:79], a[12:15], v[64:67]\n"
  "s_bitset1_b32 m0, 16\n"
  "v_mfma_f32_16x16x32_bf16 v[72:75], a[64:67], a[16:19], 0\n"
  "v_mfma_f32_16x16x32_bf16 v[72:75], a[68:71], a[20:23], v[72:75]\n"
  "v_mfma_f32_16x16x32_bf16 v[72:75], a[72:75], a[24:27], v[72:75]\n"
  "v_mfma_f32_16x16x32_bf16 v[72:75], a[76:79], a[28:31], v[72:75]\n"
  "v_mfma_f32_16x16x32_bf16 v[68:71], a[80:83], a[0:3], 0\n"
  "v_mfma_f32_16x16x32_bf16 v[68:71], a[84:87], a[4:7], v[68:71]\n"
  "v_mfma_f32_16x16x32_bf16 v[68:71], a[88:91], a[8:11], v[68:71]\n"
  "v_mfma_f32_16x16x32_bf16 v[68:71], a[92:95], a[12:15], v[68:71]\n"
  "v_mfma_f32_16x16x32_bf16 v[76:79], a[80:83], a[16:19], 0\n"
  "v_mfma_f32_16x16x32_bf16 v[76:79], a[84:87], a[20:23], v[76:79]\n"
  "v_mfma_f32_16x16x32_bf16 v[76:79], a[88:91], a[24:27], v[76:79]\n"
  "v_mfma_f32_16x16x32_bf16 v[76:79], a[92:95], a[28:31], v[76:79]\n"
  "s_cmp_lg_u32 s93, 0\n"
  "s_cbranch_scc1 Lsp0%=\n"
  "Lbp0%=:\n"
  "v_mfma_f32_16x16x32_bf16 v[96:99], a[64:67], a[32:35], 0\n"
  "v_max3_f32 v30, v64, v65, v2\n"
  "v_max3_f32 v31, v72, v73, v3\n"
  "v_mfma_f32_16x16x32_bf16 v[96:99], a[68:71], a[36:39], v[96:99]\n"
  "v_max3_f32 v30, v30, v66, v67\n"
  "v_max3_f32 v31, v31, v74, v75\n"
  "v_mfma_f32_16x16x32_bf16 v[96:99], a[72:75], a[40:43], v[96:99]\n"
  "v_max3_f32 v30, v30, v68, v69\n"
  "v_max3_f32 v31, v31, v76, v77\n"
  "v_mfma_f32_16x16x32_bf16 v[96:99], a[76:79], a[44:47], v[96:99]\n"
  "v_max3_f32 v30, v30, v70, v71\n"
  "v_max3_f32 v31, v31, v78, v79\n"
  "v_mfma_f32_16x16x32_bf16 v[104:107], a[64:67], a[48:51], 0\n"
  "v_mfma_f32_16x16x32_bf16 v[104:107], a[68:71], a[52:55], v[104:107]\n"
  "v_permlane32_swap_b32 v30, v31\n"
  "v_max_f32 v30, v30, v31\n"
  "v_mfma_f32_16x16x32_bf16 v[104:107], a[72:75], a[56:59], v[104:107]\n"
  "v_mov_b32 v31, v30\n"
  "v_mfma_f32_16x16x32_bf16 v[104:107], a[76:79], a[60:63], v[104:107]\n"
  "v_mfma_f32_16x16x32_bf16 v[100:103], a[80:83], a[32:35], 0\n"
  "v_permlane16_swap_b32 v30, v31\n"
  "v_max_f32 v30, v30, v31\n"
  "v_mfma_f32_16x16x32_bf16 v[100:103], a[84:87], a[36:39], v[100:103]\n"
  "v_mov_b32 v31, v30\n"
  "v_mfma_f32_16x16x32_bf16 v[100:103], a[88:91], a[40:43], v[100:103]\n"
  "v_mfma_f32_16x16x32_bf16 v[100:103], a[92:95], a[44:47], v[100:103]\n"
  "v_permlane32_swap_b32 v30, v31\n"
  "v_sub_f32 v32, 0, v30\n"
  "v_mfma_f32_16x16x32_bf16 v[108:111], a[80:83], a[48:51], 0\n"
  "v_sub_f32 v33, 0, v30\n"
  "v_mov_b64 v[34:35], v[32:33]\n"
  "v_mfma_f32_16x16x32_bf16 v[108:111], a[84:87], a[52:55], v[108:111]\n"
  "v_sub_f32 v36, 0, v31\n"
  "v_sub_f32 v37, 0, v31\n"
  "v_mfma_f32_16x16x32_bf16 v[108:111], a[88:91], a[56:59], v[108:111]\n"
  "v_mov_b64 v[38:39], v[36:37]\n"
  "v_add_f32 v64, v32, v64\n"
  "v_mfma_f32_16x16x32_bf16 v[108:111], a[92:95], a[60:63], v[108:111]\n"
  "s_waitcnt vmcnt(8)\n"
  "s_barrier\n"
  "v_mfma_f32_16x16x32_bf16 v[80:83], a[96:99], a[0:3], v[32:35]\n"
  "buffer_load_dwordx4 v18, s[48:51], s67 offen offset:0 nt lds\n"
  "v_mfma_f32_16x16x32_bf16 v[80:83], a[100:103], a[4:7], v[80:83]\n"
  "buffer_load_dwordx4 v19, s[48:51], s67 offen offset:1024 nt lds\n"
  "s_waitcnt lgkmcnt(0)\n"
  "v_mfma_f32_16x16x32_bf16 v[80:83], a[104:107], a[8:11], v[80:83]\n"
  "ds_read_b64_tr_b16 a[128:129], v12 offset:0\n"
  "v_mfma_f32_16x16x32_bf16 v[80:83], a[108:111], a[12:15], v[80:83]\n"
  "ds_read_b64_tr_b16 a[130:131], v12 offset:4096\n"
  "v_lshl_or_b32 v20, v20, 14, v29\n"
  "v_mfma_f32_16x16x32_bf16 v[88:91], a[96:99], a[16:19], v[36:39]\n"
  "ds_read_b64_tr_b16 a[160:161], v12 offset:8192\n"
  "s_cmp_lg_u32 s93, 0\n"
  "s_cbranch_scc1 Lsp2%=\n"
  "Lbp2%=:\n"
  "v_mfma_f32_16x16x32_bf16 v[88:91], a[100:103], a[20:23], v[88:91]\n"
  "ds_read_b64_tr_b16 a[162:163], v12 offset:12288\n"
  "v_max3_f32 v30, v96, v97, v4\n"
  "v_mfma_f32_16x16x32_bf16 v[88:91], a[104:107], a[24:27], v[88:91]\n"
  "ds_read_b64_tr_b16 a[132:133], v13 offset:0\n"
  "v_max3_f32 v31, v104, v105, v5\n"
  "v_mfma_f32_16x16x32_bf16 v[88:91], a[108:111], a[28:31], v[88:91]\n"
  "ds_read_b64_tr_b16 a[134:135], v13 offset:4096\n"
  "v_max3_f32 v30, v30, v98, v99\n"
  "v_mfma_f32_16x16x32_bf16 v[84:87], a[112:115], a[0:3], v[32:35]\n"
  "ds_read_b64_tr_b16 a[164:165], v13 offset:8192\n"
  "v_max3_f32 v31, v31, v106, v107\n"
  "v_mfma_f32_16x16x32_bf16 v[84:87], a[116:119], a[4:7], v[84:87]\n"
  "ds_read_b64_tr_b16 a[166:167], v13 offset:12288\n"
  "v_max3_f32 v30, v30, v100, v101\n"
  "v_mfma_f32_16x16x32_bf16 v[84:87], a[120:123], a[8:11], v[84:87]\n"
  "ds_read_b64_tr_b16 a[136:137], v12 offset:256\n"
  "v_max3_f32 v31, v31, v108, v109\n"
  "v_mfma_f32_16x16x32_bf16 v[84:87], a[124:127], a[12:15], v[84:87]\n"
  "ds_read_b64_tr_b16 a[138:139], v12 offset:4352\n"
  "v_max3_f32 v30, v30, v102, v103\n"
  "v_mfma_f32_16x16x32_bf16 v[92:95], a[112:115], a[16:19], v[36:39]\n"
  "ds_read_b64_tr_b16 a[168:169], v12 offset:8448\n"
  "v_max3_f32 v31, v31, v110, v111\n"
  "v_mfma_f32_16x16x32_bf16 v[92:95], a[116:119], a[20:23], v[92:95]\n"
  "ds_read_b64_tr_b16 a[170:171], v12 offset:12544\n"
  "v_permlane32_swap_b32 v30, v31\n"
  "v_mfma_f32_16x16x32_bf16 v[92:95], a[120:123], a[24:27], v[92:95]\n"
  "buffer_load_dwordx4 v18, s[48:51], s67 offen offset:2048 nt lds\n"
  "v_mfma_f32_16x16x32_bf16 v[92:95], a[124:127], a[28:31], v[92:95]\n"
  "ds_read_b64_tr_b16 a[140:141], v13 offset:256\n"
  "v_max_f32 v30, v30, v31\n"
  "v_mov_b32 v31, v30\n"
  "v_add_f32 v65, v32, v65\n"
  "v_add_f32 v66, v32, v66\n"
  "v_permlane16_swap_b32 v30, v31\n"
  "v_max_f32 v30, v30, v31\n"
  "v_mov_b32 v31, v30\n"
  "v_add_f32 v67, v32, v67\n"
  "v_add_f32 v68, v32, v68\n"
  "v_permlane32_swap_b32 v30, v31\n"
  "v_sub_f32 v40, 0, v30\n"
  "v_sub_f32 v41, 0, v30\n"
  "v_mov_b64 v[42:43], v[40:41]\n"
  "v_sub_f32 v44, 0, v31\n"
  "v_sub_f32 v45, 0, v31\n"
  "v_mfma_f32_16x16x32_bf16 v[112:115], a[96:99], a[32:35], v[40:43]\n"
  "ds_read_b64_tr_b16 a[142:143], v13 offset:4352\n"
  "v_mov_b64 v[46:47], v[44:45]\n"
  "v_mfma_f32_16x16x32_bf16 v[112:115], a[100:103], a[36:39], v[112:115]\n"
  "ds_read_b64_tr_b16 a[172:173], v13 offset:8448\n"
  "v_add_f32 v69, v32, v69\n"
  "v_mfma_f32_16x16x32_bf16 v[112:115], a[104:107], a[40:43], v[112:115]\n"
  "ds_read_b64_tr_b16 a[174:175], v13 offset:12544\n"
  "v_add_f32 v70, v32, v70\n"
  "v_mfma_f32_16x16x32_bf16 v[112:115], a[108:111], a[44:47], v[112:115]\n"
  "ds_read_b64_tr_b16 a[144:145], v12 offset:512\n"
  "v_add_f32 v71, v32, v71\n"
  "v_mfma_f32_16x16x32_bf16 v[120:123], a[96:99], a[48:51], v[44:47]\n"
  "ds_read_b64_tr_b16 a[146:147], v12 offset:4608\n"
  "v_add_f32 v72, v36, v72\n"
  "v_mfma_f32_16x16x32_bf16 v[120:123], a[100:103], a[52:55], v[120:123]\n"
  "ds_read_b64_tr_b16 a[176:177], v12 offset:8704\n"
  "v_add_f32 v73, v36, v73\n"
  "v_mfma_f32_16x16x32_bf16 v[120:123], a[104:107], a[56:59], v[120:123]\n"
  "ds_read_b64_tr_b16 a[178:179], v12 offset:12800\n"
  "v_add_f32 v74, v36, v74\n"
  "v_mfma_f32_16x16x32_bf16 v[120:123], a[108:111], a[60:63], v[120:123]\n"
  "ds_read_b64_tr_b16 a[148:149], v13 offset:512\n"
  "v_add_f32 v75, v36, v75\n"
  "v_mfma_f32_16x16x32_bf16 v[116:119], a[112:115], a[32:35], v[40:43]\n"
  "buffer_load_dwordx4 v19, s[48:51], s67 offen offset:3072 nt lds\n"
  "v_mfma_f32_16x16x32_bf16 v[116:119], a[116:119], a[36:39], v[116:119]\n"
  "ds_read_b64_tr_b16 a[150:151], v13 offset:4608\n"
  "v_add_f32 v76, v36, v76\n"
  "v_mfma_f32_16x16x32_bf16 v[116:119], a[120:123], a[40:43], v[116:119]\n"
  "ds_read_b64_tr_b16 a[180:181], v13 offset:8704\n"
  "v_add_f32 v77, v36, v77\n"
  "v_mfma_f32_16x16x32_bf16 v[116:119], a[124:127], a[44:47], v[116:119]\n"
  "ds_read_b64_tr_b16 a[182:183], v13 offset:12800\n"
  "v_add_f32 v78, v36, v78\n"
  "v_mfma_f32_16x16x32_bf16 v[124:127], a[112:115], a[48:51], v[44:47]\n"
  "ds_read_b64_tr_b16 a[152:153], v12 offset:768\n"
  "v_add_f32 v79, v36, v79\n"
  "v_mfma_f32_16x16x32_bf16 v[124:127], a[116:119], a[52:55], v[124:127]\n"
  "ds_read_b64_tr_b16 a[154:155], v12 offset:4864\n"
  "s_cmp_lg_u32 s93, 0\n"
  "s_cbranch_scc1 Lsp1%=\n"
  "Lbp1%=:\n"
  "v_mfma_f32_16x16x32_bf16 v[124:127], a[120:123], a[56:59], v[124:127]\n"
  "ds_read_b64_tr_b16 a[184:185], v12 offset:8960\n"
  "v_add_f32 v96, v40, v96\n"
  "v_mfma_f32_16x16x32_bf16 v[124:127], a[124:127], a[60:63], v[124:127]\n"
  "ds_read_b64_tr_b16 a[186:187], v12 offset:13056\n"
  "v_add_f32 v97, v40, v97\n"
  "v_add_f32 v98, v40, v98\n"
  "v_add_f32 v99, v40, v99\n"
  "v_add_f32 v100, v40, v100\n"
  "v_add_f32 v101, v40, v101\n"
  "v_add_f32 v102, v40, v102\n"
  "v_add_f32 v103, v40, v103\n"
  "v_add_f32 v104, v44, v104\n"
  "v_add_f32 v105, v44, v105\n"
  "v_add_f32 v106, v44, v106\n"
  "v_add_f32 v107, v44, v107\n"
  "v_add_f32 v108, v44, v108\n"
  "v_add_f32 v109, v44, v109\n"
  "v_add_f32 v110, v44, v110\n"
  "v_add_f32 v111, v44, v111\n"
  "v_exp_f32 v64, v64\n"
  "v_exp_f32 v65, v65\n"
  "v_exp_f32 v66, v66\n"
  "v_cvt_pk_bf16_f32 v64, v64, v65\n"
  "v_exp_f32 v67, v67\n"
  "v_exp_f32 v68, v68\n"
  "v_cvt_pk_bf16_f32 v65, v66, v67\n"
  "v_exp_f32 v69, v69\n"
  "v_exp_f32 v70, v70\n"
  "v_cvt_pk_bf16_f32 v66, v68, v69\n"
  "v_exp_f32 v71, v71\n"
  "v_exp_f32 v80, v80\n"
  "v_exp_f32 v81, v81\n"
  "v_exp_f32 v82, v82\n"
  "v_exp_f32 v83, v83\n"
  "v_exp_f32 v84, v84\n"
  "v_exp_f32 v85, v85\n"
  "v_exp_f32 v86, v86\n"
  "v_exp_f32 v87, v87\n"
  "v_exp_f32 v72, v72\n"
  "v_exp_f32 v73, v73\n"
  "v_cvt_pk_bf16_f32 v67, v70, v71\n"
  "v_exp_f32 v74, v74\n"
  "s_add_u32 s66, s66, 0x4000\n"
  "v_readlane_b32 s67, v20, s65\n"
  "v_exp_f32 v75, v75\n"
  "s_and_b32 m0, s66, 0xffff\n"
  "s_add_u32 s65, s65, 1\n"
  "v_exp_f32 v76, v76\n"
  "v_cvt_pk_bf16_f32 v80, v80, v81\n"
  "v_exp_f32 v77, v77\n"
  "v_exp_f32 v78, v78\n"
  "v_cvt_pk_bf16_f32 v81, v82, v83\n"
  "v_exp_f32 v79, v79\n"
  "v_cvt_pk_bf16_f32 v82, v84, v85\n"
  "v_exp_f32 v88, v88\n"
  "v_cvt_pk_bf16_f32 v83, v86, v87\n"
  "buffer_load_dwordx4 v14, s[44:47], s67 offen offset:0 nt lds\n"
  "v_exp_f32 v89, v89\n"
  "v_cvt_pk_bf16_f32 v72, v72, v73\n"
  "v_exp_f32 v90, v90\n"
  "v_cvt_pk_bf16_f32 v73, v74, v75\n"
  "v_exp_f32 v91, v91\n"
  "v_cvt_pk_bf16_f32 v74, v76, v77\n"
  "v_exp_f32 v92, v92\n"
  "ds_read_b64_tr_b16 a[156:157], v13 offset:768\n"
  "v_cvt_pk_bf16_f32 v75, v78, v79\n"
  "v_exp_f32 v93, v93\n"
  "ds_read_b64_tr_b16 a[158:159], v13 offset:4864\n"
  "v_cvt_pk_bf16_f32 v88, v88, v89\n"
  "v_exp_f32 v94, v94\n"
  "v_cvt_pk_bf16_f32 v89, v90, v91\n"
  "s_waitcnt lgkmcnt(2)\n"
  "s_cmp_lt_u32 s65, s64\n"
  "s_cbranch_scc0 Lfinal%=\n"
  "Lloop1%=:\n"
  "v_mfma_f32_16x16x32_bf16 v[128:131], a[128:131], v[64:67], v[128:131]\n"
  "v_exp_f32 v95, v95\n"
  "v_mfma_f32_16x16x32_bf16 v[128:131], a[160:163], v[80:83], v[128:131]\n"
  "ds_read_b64_tr_b16 a[188:189], v13 offset:8960\n"
  "v_cvt_pk_bf16_f32 v90, v92, v93\n"
  "v_mfma_f32_16x16x32_bf16 v[132:135], a[132:135], v[64:67], v[132:135]\n"
  "v_exp_f32 v96, v96\n"
  "v_mfma_f32_16x16x32_bf16 v[132:135], a[164:167], v[80:83], v[132:135]\n"
  "ds_read_b64_tr_b16 a[190:191], v13 offset:13056\n"
  "v_cvt_pk_bf16_f32 v91, v94, v95\n"
  "v_mfma_f32_16x16x32_bf16 v[136:139], a[136:139], v[64:67], v[136:139]\n"
  "v_exp_f32 v97, v97\n"
  "v_mfma_f32_16x16x32_bf16 v[136:139], a[168:171], v[80:83], v[136:139]\n"
  "buffer_load_dwordx4 v15, s[44:47], s67 offen offset:1024 nt lds\n"
  "s_nop 7\n"
  "s_nop 5\n"
  "v_mfma_f32_16x16x32_bf16 v[140:143], a[140:143], v[64:67], v[140:143]\n"
  "ds_read_b128 a[64:67], v8 offset:0\n"
  "v_xor_b32 v12, s69, v12\n"
  "v_mfma_f32_16x16x32_bf16 v[140:143], a[172:175], v[80:83], v[140:143]\n"
  "v_exp_f32 v98, v98\n"
  "v_mfma_f32_16x16x32_bf16 v[144:147], a[144:147], v[64:67], v[144:147]\n"
  "ds_read_b128 a[68:71], v9 offset:0\n"
  "v_xor_b32 v13, s69, v13\n"
  "v_mfma_f32_16x16x32_bf16 v[144:147], a[176:179], v[80:83], v[144:147]\n"
  "v_exp_f32 v99, v99\n"
  "v_mfma_f32_16x16x32_bf16 v[148:151], a[148:151], v[64:67], v[148:151]\n"
  "s_xor_b32 s69, s69, 0x8000\n"
  "ds_read_b128 a[72:75], v10 offset:0\n"
  "v_mfma_f32_16x16x32_bf16 v[148:151], a[180:183], v[80:83], v[148:151]\n"
  "v_exp_f32 v100, v100\n"
  "v_mfma_f32_16x16x32_bf16 v[152:155], a[152:155], v[64:67], v[152:155]\n"
  "s_waitcnt lgkmcnt(3)\n"
  "ds_read_b128 a[76:79], v11 offset:0\n"
  "v_mfma_f32_16x16x32_bf16 v[152:155], a[184:187], v[80:83], v[152:155]\n"
  "v_exp_f32 v101, v101\n"
  "s_nop 7\n"
  "s_nop 5\n"
  "v_mfma_f32_16x16x32_bf16 v[156:159], a[156:159], v[64:67], v[156:159]\n"
  "ds_read_b128 a[80:83], v8 offset:4096\n"
  "v_cvt_pk_bf16_f32 v96, v96, v97\n"
  "v_mfma_f32_16x16x32_bf16 v[156:159], a[188:191], v[80:83], v[156:159]\n"
  "v_exp_f32 v102, v102\n"
  "v_mfma_f32_16x16x32_bf16 v[48:51], a[192:195], v[64:67], v[48:51]\n"
  "ds_read_b128 a[84:87], v9 offset:4096\n"
  "v_cvt_pk_bf16_f32 v97, v98, v99\n"
  "v_mfma_f32_16x16x32_bf16 v[48:51], a[192:195], v[80:83], v[48:51]\n"
  "buffer_load_dwordx4 v16, s[44:47], s67 offen offset:2048 nt lds\n"
  "v_mfma_f32_16x16x32_bf16 v[160:163], a[128:131], v[72:75], v[160:163]\n"
  "v_exp_f32 v103, v103\n"
  "v_mfma_f32_16x16x32_bf16 v[160:163], a[160:163], v[88:91], v[160:163]\n"
  "ds_read_b128 a[88:91], v10 offset:4096\n"
  "v_cvt_pk_bf16_f32 v98, v100, v101\n"
  "v_mfma_f32_16x16x32_bf16 v[164:167], a[132:135], v[72:75], v[164:167]\n"
  "v_exp_f32 v112, v112\n"
  "v_mfma_f32_16x16x32_bf16 v[164:167], a[164:167], v[88:91], v[164:167]\n"
  "ds_read_b128 a[92:95], v11 offset:4096\n"
  "v_cvt_pk_bf16_f32 v99, v102, v103\n"
  "s_nop 7\n"
  "s_nop 5\n"
  "v_mfma_f32_16x16x32_bf16 v[168:171], a[136:139], v[72:75], v[168:171]\n"
  "v_exp_f32 v113, v113\n"
  "v_mfma_f32_16x16x32_bf16 v[168:171], a[168:171], v[88:91], v[168:171]\n"
  "ds_read_b128 a[96:99], v8 offset:8192\n"
  "v_cvt_pk_bf16_f32 v112, v112, v113\n"
  "v_mfma_f32_16x16x32_bf16 v[172:175], a[140:143], v[72:75], v[172:175]\n"
  "v_exp_f32 v114, v114\n"
  "v_mfma_f32_16x16x32_bf16 v[172:175], a[172:175], v[88:91], v[172:175]\n"
  "v_exp_f32 v115, v115\n"
  "v_mfma_f32_16x16x32_bf16 v[176:179], a[144:147], v[72:75], v[176:179]\n"
  "ds_read_b128 a[100:103], v9 offset:8192\n"
  "v_cvt_pk_bf16_f32 v113, v114, v115\n"
  "v_mfma_f32_16x16x32_bf16 v[176:179], a[176:179], v[88:91], v[176:179]\n"
  "v_exp_f32 v116, v116\n"
  "v_mfma_f32_16x16x32_bf16 v[180:183], a[148:151], v[72:75], v[180:183]\n"
  "ds_read_b128 a[104:107], v10 offset:8192\n"
  "v_mfma_f32_16x16x32_bf16 v[180:183], a[180:183], v[88:91], v[180:183]\n"
  "buffer_load_dwordx4 v17, s[44:47], s67 offen offset:3072 nt lds\n"
  "s_nop 7\n"
  "s_nop 5\n"
  "v_mfma_f32_16x16x32_bf16 v[184:187], a[152:155], v[72:75], v[184:187]\n"
  "v_exp_f32 v117, v117\n"
  "v_mfma_f32_16x16x32_bf16 v[184:187], a[184:187], v[88:91], v[184:187]\n"
  "ds_read_b128 a[108:111], v11 offset:8192\n"
  "v_cvt_pk_bf16_f32 v114, v116, v117\n"
  "v_mfma_f32_16x16x32_bf16 v[188:191], a[156:159], v[72:75], v[188:191]\n"
  "v_exp_f32 v118, v118\n"
  "v_mfma_f32_16x16x32_bf16 v[188:191], a[188:191], v[88:91], v[188:191]\n"
  "ds_read_b128 a[112:115], v8 offset:12288\n"
  "v_mfma_f32_16x16x32_bf16 v[52:55], a[192:195], v[72:75], v[52:55]\n"
  "v_exp_f32 v119, v119\n"
  "v_mfma_f32_16x16x32_bf16 v[52:55], a[192:195], v[88:91], v[52:55]\n"
  "s_waitcnt lgkmcnt(2)\n"
  "ds_read_b128 a[116:119], v9 offset:12288\n"
  "v_mfma_f32_16x16x32_bf16 v[64:67], a[64:67], a[0:3], v[32:35]\n"
  "v_exp_f32 v104, v104\n"
  "v_mfma_f32_16x16x32_bf16 v[64:67], a[68:71], a[4:7], v[64:67]\n"
  "ds_read_b128 a[120:123], v10 offset:12288\n"
  "v_cvt_pk_bf16_f32 v115, v118, v119\n"
  "s_nop 7\n"
  "s_nop 5\n"
  "v_mfma_f32_16x16x32_bf16 v[64:67], a[72:75], a[8:11], v[64:67]\n"
  "v_exp_f32 v105, v105\n"
  "v_mfma_f32_16x16x32_bf16 v[64:67], a[76:79], a[12:15], v[64:67]\n"
  "ds_read_b128 a[124:127], v11 offset:12288\n"
  "v_cvt_pk_bf16_f32 v104, v104, v105\n"
  "v_mfma_f32_16x16x32_bf16 v[72:75], a[64:67], a[16:19], v[36:39]\n"
  "v_exp_f32 v106, v106\n"
  "v_mfma_f32_16x16x32_bf16 v[72:75], a[68:71], a[20:23], v[72:75]\n"
  "v_xor_b32 v8, s69, v8\n"
  "v_mfma_f32_16x16x32_bf16 v[72:75], a[72:75], a[24:27], v[72:75]\n"
  "v_exp_f32 v107, v107\n"
  "v_mfma_f32_16x16x32_bf16 v[72:75], a[76:79], a[28:31], v[72:75]\n"
  "v_xor_b32 v9, s69, v9\n"
  "v_cvt_pk_bf16_f32 v105, v106, v107\n"
  "v_mfma_f32_16x16x32_bf16 v[68:71], a[80:83], a[0:3], v[32:35]\n"
  "v_exp_f32 v108, v108\n"
  "v_mfma_f32_16x16x32_bf16 v[68:71], a[84:87], a[4:7], v[68:71]\n"
  "v_xor_b32 v10, s69, v10\n"
  "s_nop 7\n"
  "s_nop 5\n"
  "v_mfma_f32_16x16x32_bf16 v[68:71], a[88:91], a[8:11], v[68:71]\n"
  "v_exp_f32 v109, v109\n"
  "v_mfma_f32_16x16x32_bf16 v[68:71], a[92:95], a[12:15], v[68:71]\n"
  "v_xor_b32 v11, s69, v11\n"
  "v_cvt_pk_bf16_f32 v106, v108, v109\n"
  "v_mfma_f32_16x16x32_bf16 v[76:79], a[80:83], a[16:19], v[36:39]\n"
  "v_exp_f32 v110, v110\n"
  "v_mfma_f32_16x16x32_bf16 v[76:79], a[84:87], a[20:23], v[76:79]\n"
  "v_mfma_f32_16x16x32_bf16 v[76:79], a[88:91], a[24:27], v[76:79]\n"
  "v_exp_f32 v111, v111\n"
  "v_mfma_f32_16x16x32_bf16 v[76:79], a[92:95], a[28:31], v[76:79]\n"
  "v_cvt_pk_bf16_f32 v107, v110, v111\n"
  "v_mfma_f32_16x16x32_bf16 v[80:83], a[96:99], a[0:3], v[32:35]\n"
  "v_exp_f32 v120, v120\n"
  "v_mfma_f32_16x16x32_bf16 v[80:83], a[100:103], a[4:7], v[80:83]\n"
  "s_waitcnt lgkmcnt(0)\n"
  "s_nop 7\n"
  "s_nop 5\n"
  "v_mfma_f32_16x16x32_bf16 v[80:83], a[104:107], a[8:11], v[80:83]\n"
  "v_exp_f32 v121, v121\n"
  "v_mfma_f32_16x16x32_bf16 v[80:83], a[108:111], a[12:15], v[80:83]\n"
  "v_cvt_pk_bf16_f32 v120, v120, v121\n"
  "v_mfma_f32_16x16x32_bf16 v[88:91], a[96:99], a[16:19], v[36:39]\n"
  "v_exp_f32 v122, v122\n"
  "v_mfma_f32_16x16x32_bf16 v[88:91], a[100:103], a[20:23], v[88:91]\n"
  "v_mfma_f32_16x16x32_bf16 v[88:91], a[104:107], a[24:27], v[88:91]\n"
  "v_exp_f32 v123, v123\n"
  "v_mfma_f32_16x16x32_bf16 v[88:91], a[108:111], a[28:31], v[88:91]\n"
  "v_cvt_pk_bf16_f32 v121, v122, v123\n"
  "v_mfma_f32_16x16x32_bf16 v[84:87], a[112:115], a[0:3], v[32:35]\n"
  "v_exp_f32 v124, v124\n"
  "v_mfma_f32_16x16x32_bf16 v[84:87], a[116:119], a[4:7], v[84:87]\n"
  "s_nop 7\n"
  "s_nop 5\n"
  "v_mfma_f32_16x16x32_bf16 v[84:87], a[120:123], a[8:11], v[84:87]\n"
  "v_exp_f32 v125, v125\n"
  "v_mfma_f32_16x16x32_bf16 v[84:87], a[124:127], a[12:15], v[84:87]\n"
  "v_cvt_pk_bf16_f32 v122, v124, v125\n"
  "v_mfma_f32_16x16x32_bf16 v[92:95], a[112:115], a[16:19], v[36:39]\n"
  "v_exp_f32 v126, v126\n"
  "v_mfma_f32_16x16x32_bf16 v[92:95], a[116:119], a[20:23], v[92:95]\n"
  "v_mfma_f32_16x16x32_bf16 v[92:95], a[120:123], a[24:27], v[92:95]\n"
  "v_exp_f32 v127, v127\n"
  "v_mfma_f32_16x16x32_bf16 v[92:95], a[124:127], a[28:31], v[92:95]\n"
  "v_cvt_pk_bf16_f32 v123, v126, v127\n"
  "s_cmp_eq_u32 s65, s68\n"
  "v_mfma_f32_16x16x32_bf16 v[192:195], a[128:131], v[96:99], v[192:195]\n"
  "s_waitcnt vmcnt(8) lgkmcnt(0)\n"
  "s_barrier\n"
  "v_mfma_f32_16x16x32_bf16 v[192:195], a[160:163], v[112:115], v[192:195]\n"
  "s_cbranch_scc1 Lsm1%=\n"
  "Lbm1%=:\n"
  "s_nop 7\n"
  "s_nop 5\n"
  "v_mfma_f32_16x16x32_bf16 v[196:199], a[132:135], v[96:99], v[196:199]\n"
  "v_exp_f32 v64, v64\n"
  "v_mfma_f32_16x16x32_bf16 v[196:199], a[164:167], v[112:115], v[196:199]\n"
  "v_exp_f32 v65, v65\n"
  "v_mfma_f32_16x16x32_bf16 v[200:203], a[136:139], v[96:99], v[200:203]\n"
  "s_bitset1_b32 m0, 16\n"
  "v_cvt_pk_bf16_f32 v64, v64, v65\n"
  "v_mfma_f32_16x16x32_bf16 v[200:203], a[168:171], v[112:115], v[200:203]\n"
  "buffer_load_dwordx4 v18, s[48:51], s67 offen offset:0 nt lds\n"
  "v_mfma_f32_16x16x32_bf16 v[204:207], a[140:143], v[96:99], v[204:207]\n"
  "v_exp_f32 v66, v66\n"
  "v_mfma_f32_16x16x32_bf16 v[204:207], a[172:175], v[112:115], v[204:207]\n"
  "v_mfma_f32_16x16x32_bf16 v[208:211], a[144:147], v[96:99], v[208:211]\n"
  "v_exp_f32 v67, v67\n"
  "v_mfma_f32_16x16x32_bf16 v[208:211], a[176:179], v[112:115], v[208:211]\n"
  "v_cvt_pk_bf16_f32 v65, v66, v67\n"
  "s_nop 7\n"
  "s_nop 5\n"
  "v_mfma_f32_16x16x32_bf16 v[212:215], a[148:151], v[96:99], v[212:215]\n"
  "v_exp_f32 v68, v68\n"
  "v_mfma_f32_16x16x32_bf16 v[212:215], a[180:183], v[112:115], v[212:215]\n"
  "v_mfma_f32_16x16x32_bf16 v[216:219], a[152:155], v[96:99], v[216:219]\n"
  "v_exp_f32 v69, v69\n"
  "v_mfma_f32_16x16x32_bf16 v[216:219], a[184:187], v[112:115], v[216:219]\n"
  "v_cvt_pk_bf16_f32 v66, v68, v69\n"
  "v_mfma_f32_16x16x32_bf16 v[220:223], a[156:159], v[96:99], v[220:223]\n"
  "v_exp_f32 v70, v70\n"
  "v_mfma_f32_16x16x32_bf16 v[220:223], a[188:191], v[112:115], v[220:223]\n"
  "v_mfma_f32_16x16x32_bf16 v[56:59], a[192:195], v[96:99], v[56:59]\n"
  "v_exp_f32 v71, v71\n"
  "v_mfma_f32_16x16x32_bf16 v[56:59], a[192:195], v[112:115], v[56:59]\n"
  "buffer_load_dwordx4 v19, s[48:51], s67 offen offset:1024 nt lds\n"
  "s_nop 7\n"
  "s_nop 5\n"
  "v_mfma_f32_16x16x32_bf16 v[224:227], a[128:131], v[104:107], v[224:227]\n"
  "ds_read_b64_tr_b16 a[128:129], v12 offset:0\n"
  "ds_read_b64_tr_b16 a[130:131], v12 offset:4096\n"
  "v_mfma_f32_16x16x32_bf16 v[224:227], a[160:163], v[120:123], v[224:227]\n"
  "v_exp_f32 v80, v80\n"
  "v_mfma_f32_16x16x32_bf16 v[228:231], a[132:135], v[104:107], v[228:231]\n"
  "ds_read_b64_tr_b16 a[160:161], v12 offset:8192\n"
  "ds_read_b64_tr_b16 a[162:163], v12 offset:12288\n"
  "v_mfma_f32_16x16x32_bf16 v[228:231], a[164:167], v[120:123], v[228:231]\n"
  "v_exp_f32 v81, v81\n"
  "v_mfma_f32_16x16x32_bf16 v[232:235], a[136:139], v[104:107], v[232:235]\n"
  "ds_read_b64_tr_b16 a[132:133], v13 offset:0\n"
  "ds_read_b64_tr_b16 a[134:135], v13 offset:4096\n"
  "v_mfma_f32_16x16x32_bf16 v[232:235], a[168:171], v[120:123], v[232:235]\n"
  "v_exp_f32 v82, v82\n"
  "v_mfma_f32_16x16x32_bf16 v[236:239], a[140:143], v[104:107], v[236:239]\n"
  "ds_read_b64_tr_b16 a[164:165], v13 offset:8192\n"
  "ds_read_b64_tr_b16 a[166:167], v13 offset:12288\n"
  "v_mfma_f32_16x16x32_bf16 v[236:239], a[172:175], v[120:123], v[236:239]\n"
  "v_exp_f32 v83, v83\n"
  "s_nop 7\n"
  "s_nop 5\n"
  "v_mfma_f32_16x16x32_bf16 v[240:243], a[144:147], v[104:107], v[240:243]\n"
  "ds_read_b64_tr_b16 a[136:137], v12 offset:256\n"
  "ds_read_b64_tr_b16 a[138:139], v12 offset:4352\n"
  "v_mfma_f32_16x16x32_bf16 v[240:243], a[176:179], v[120:123], v[240:243]\n"
  "v_exp_f32 v84, v84\n"
  "v_mfma_f32_16x16x32_bf16 v[244:247], a[148:151], v[104:107], v[244:247]\n"
  "ds_read_b64_tr_b16 a[168:169], v12 offset:8448\n"
  "ds_read_b64_tr_b16 a[170:171], v12 offset:12544\n"
  "v_mfma_f32_16x16x32_bf16 v[244:247], a[180:183], v[120:123], v[244:247]\n"
  "buffer_load_dwordx4 v18, s[48:51], s67 offen offset:2048 nt lds\n"
  "v_mfma_f32_16x16x32_bf16 v[248:251], a[152:155], v[104:107], v[248:251]\n"
  "v_exp_f32 v85, v85\n"
  "v_mfma_f32_16x16x32_bf16 v[248:251], a[184:187], v[120:123], v[248:251]\n"
  "ds_read_b64_tr_b16 a[140:141], v13 offset:256\n"
  "ds_read_b64_tr_b16 a[142:143], v13 offset:4352\n"
  "v_mfma_f32_16x16x32_bf16 v[252:255], a[156:159], v[104:107], v[252:255]\n"
  "v_exp_f32 v86, v86\n"
  "v_mfma_f32_16x16x32_bf16 v[252:255], a[188:191], v[120:123], v[252:255]\n"
  "ds_read_b64_tr_b16 a[172:173], v13 offset:8448\n"
  "ds_read_b64_tr_b16 a[174:175], v13 offset:12544\n"
  "s_nop 7\n"
  "s_nop 5\n"
  "v_mfma_f32_16x16x32_bf16 v[60:63], a[192:195], v[104:107], v[60:63]\n"
  "v_exp_f32 v87, v87\n"
  "v_mfma_f32_16x16x32_bf16 v[60:63], a[192:195], v[120:123], v[60:63]\n"
  "ds_read_b64_tr_b16 a[144:145], v12 offset:512\n"
  "ds_read_b64_tr_b16 a[146:147], v12 offset:4608\n"
  "v_mfma_f32_16x16x32_bf16 v[96:99], a[64:67], a[32:35], v[40:43]\n"
  "v_exp_f32 v72, v72\n"
  "v_mfma_f32_16x16x32_bf16 v[96:99], a[68:71], a[36:39], v[96:99]\n"
  "ds_read_b64_tr_b16 a[176:177], v12 offset:8704\n"
  "ds_read_b64_tr_b16 a[178:179], v12 offset:12800\n"
  "v_mfma_f32_16x16x32_bf16 v[96:99], a[72:75], a[40:43], v[96:99]\n"
  "v_exp_f32 v73, v73\n"
  "v_mfma_f32_16x16x32_bf16 v[96:99], a[76:79], a[44:47], v[96:99]\n"
  "ds_read_b64_tr_b16 a[148:149], v13 offset:512\n"
  "v_cvt_pk_bf16_f32 v67, v70, v71\n"
  "v_mfma_f32_16x16x32_bf16 v[104:107], a[64:67], a[48:51], v[44:47]\n"
  "v_exp_f32 v74, v74\n"
  "v_mfma_f32_16x16x32_bf16 v[104:107], a[68:71], a[52:55], v[104:107]\n"
  "buffer_load_dwordx4 v19, s[48:51], s67 offen offset:3072 nt lds\n"
  "s_nop 7\n"
  "s_nop 5\n"
  "v_mfma_f32_16x16x32_bf16 v[104:107], a[72:75], a[56:59], v[104:107]\n"
  "s_add_u32 s66, s66, 0x4000\n"
  "v_readlane_b32 s67, v20, s65\n"
  "v_mfma_f32_16x16x32_bf16 v[104:107], a[76:79], a[60:63], v[104:107]\n"
  "v_exp_f32 v75, v75\n"
  "v_mfma_f32_16x16x32_bf16 v[100:103], a[80:83], a[32:35], v[40:43]\n"
  "s_and_b32 m0, s66, 0xffff\n"
  "s_add_u32 s65, s65, 1\n"
  "v_mfma_f32_16x16x32_bf16 v[100:103], a[84:87], a[36:39], v[100:103]\n"
  "v_exp_f32 v76, v76\n"
  "v_mfma_f32_16x16x32_bf16 v[100:103], a[88:91], a[40:43], v[100:103]\n"
  "ds_read_b64_tr_b16 a[150:151], v13 offset:4608\n"
  "v_cvt_pk_bf16_f32 v80, v80, v81\n"
  "v_mfma_f32_16x16x32_bf16 v[100:103], a[92:95], a[44:47], v[100:103]\n"
  "v_exp_f32 v77, v77\n"
  "v_mfma_f32_16x16x32_bf16 v[108:111], a[80:83], a[48:51], v[44:47]\n"
  "v_exp_f32 v78, v78\n"
  "v_mfma_f32_16x16x32_bf16 v[108:111], a[84:87], a[52:55], v[108:111]\n"
  "ds_read_b64_tr_b16 a[180:181], v13 offset:8704\n"
  "v_cvt_pk_bf16_f32 v81, v82, v83\n"
  "s_nop 7\n"
  "s_nop 5\n"
  "v_mfma_f32_16x16x32_bf16 v[108:111], a[88:91], a[56:59], v[108:111]\n"
  "v_exp_f32 v79, v79\n"
  "v_mfma_f32_16x16x32_bf16 v[108:111], a[92:95], a[60:63], v[108:111]\n"
  "ds_read_b64_tr_b16 a[182:183], v13 offset:12800\n"
  "v_cvt_pk_bf16_f32 v82, v84, v85\n"
  "v_mfma_f32_16x16x32_bf16 v[112:115], a[96:99], a[32:35], v[40:43]\n"
  "v_exp_f32 v88, v88\n"
  "v_mfma_f32_16x16x32_bf16 v[112:115], a[100:103], a[36:39], v[112:115]\n"
  "ds_read_b64_tr_b16 a[152:153], v12 offset:768\n"
  "v_cvt_pk_bf16_f32 v83, v86, v87\n"
  "v_mfma_f32_16x16x32_bf16 v[112:115], a[104:107], a[40:43], v[112:115]\n"
  "buffer_load_dwordx4 v14, s[44:47], s67 offen offset:0 nt lds\n"
  "v_mfma_f32_16x16x32_bf16 v[112:115], a[108:111], a[44:47], v[112:115]\n"
  "v_exp_f32 v89, v89\n"
  "v_mfma_f32_16x16x32_bf16 v[120:123], a[96:99], a[48:51], v[44:47]\n"
  "ds_read_b64_tr_b16 a[154:155], v12 offset:4864\n"
  "v_cvt_pk_bf16_f32 v72, v72, v73\n"
  "v_mfma_f32_16x16x32_bf16 v[120:123], a[100:103], a[52:55], v[120:123]\n"
  "v_exp_f32 v90, v90\n"
  "s_nop 7\n"
  "s_nop 5\n"
  "v_mfma_f32_16x16x32_bf16 v[120:123], a[104:107], a[56:59], v[120:123]\n"
  "ds_read_b64_tr_b16 a[184:185], v12 offset:8960\n"
  "v_cvt_pk_bf16_f32 v73, v74, v75\n"
  "v_mfma_f32_16x16x32_bf16 v[120:123], a[108:111], a[60:63], v[120:123]\n"
  "v_exp_f32 v91, v91\n"
  "v_mfma_f32_16x16x32_bf16 v[116:119], a[112:115], a[32:35], v[40:43]\n"
  "ds_read_b64_tr_b16 a[186:187], v12 offset:13056\n"
  "v_cvt_pk_bf16_f32 v74, v76, v77\n"
  "v_mfma_f32_16x16x32_bf16 v[116:119], a[116:119], a[36:39], v[116:119]\n"
  "v_exp_f32 v92, v92\n"
  "v_mfma_f32_16x16x32_bf16 v[116:119], a[120:123], a[40:43], v[116:119]\n"
  "ds_read_b64_tr_b16 a[156:157], v13 offset:768\n"
  "v_cvt_pk_bf16_f32 v75, v78, v79\n"
  "v_mfma_f32_16x16x32_bf16 v[116:119], a[124:127], a[44:47], v[116:119]\n"
  "v_exp_f32 v93, v93\n"
  "v_mfma_f32_16x16x32_bf16 v[124:127], a[112:115], a[48:51], v[44:47]\n"
  "ds_read_b64_tr_b16 a[158:159], v13 offset:4864\n"
  "v_cvt_pk_bf16_f32 v88, v88, v89\n"
  "v_mfma_f32_16x16x32_bf16 v[124:127], a[116:119], a[52:55], v[124:127]\n"
  "v_exp_f32 v94, v94\n"
  "s_nop 7\n"
  "s_nop 5\n"
  "v_mfma_f32_16x16x32_bf16 v[124:127], a[120:123], a[56:59], v[124:127]\n"
  "v_cvt_pk_bf16_f32 v89, v90, v91\n"
  "s_cmp_lt_u32 s65, s20\n"
  "v_mfma_f32_16x16x32_bf16 v[124:127], a[124:127], a[60:63], v[124:127]\n"
  "s_waitcnt lgkmcnt(2)\n"
  "s_cbranch_scc1 Lloop1%=\n"
  "s_cmp_lt_u32 s65, s64\n"
  "s_cbranch_scc0 Lfinal%=\n"
  ".p2align 6\n"
  "Lloop0%=:\n"
  "v_mfma_f32_16x16x32_bf16 v[128:131], a[128:131], v[64:67], v[128:131]\n"
  "v_exp_f32 v95, v95\n"
  "v_mfma_f32_16x16x32_bf16 v[128:131], a[160:163], v[80:83], v[128:131]\n"
  "ds_read_b64_tr_b16 a[188:189], v13 offset:8960\n"
  "v_cvt_pk_bf16_f32 v90, v92, v93\n"
  "v_mfma_f32_16x16x32_bf16 v[132:135], a[132:135], v[64:67], v[132:135]\n"
  "v_exp_f32 v96, v96\n"
  "v_mfma_f32_16x16x32_bf16 v[132:135], a[164:167], v[80:83], v[132:135]\n"
  "ds_read_b64_tr_b16 a[190:191], v13 offset:13056\n"
  "v_cvt_pk_bf16_f32 v91, v94, v95\n"
  "v_mfma_f32_16x16x32_bf16 v[136:139], a[136:139], v[64:67], v[136:139]\n"
  "v_exp_f32 v97, v97\n"
  "v_mfma_f32_16x16x32_bf16 v[136:139], a[168:171], v[80:83], v[136:139]\n"
  "buffer_load_dwordx4 v15, s[44:47], s67 offen offset:1024 nt lds\n"
  "v_mfma_f32_16x16x32_bf16 v[140:143], a[140:143], v[64:67], v[140:143]\n"
  "ds_read_b128 a[64:67], v8 offset:0\n"
  "v_xor_b32 v12, s69, v12\n"
  "v_mfma_f32_16x16x32_bf16 v[140:143], a[172:175], v[80:83], v[140:143]\n"
  "v_exp_f32 v98, v98\n"
  "v_mfma_f32_16x16x32_bf16 v[144:147], a[144:147], v[64:67], v[144:147]\n"
  "ds_read_b128 a[68:71], v9 offset:0\n"
  "v_xor_b32 v13, s69, v13\n"
  "v_mfma_f32_16x16x32_bf16 v[144:147], a[176:179], v[80:83], v[144:147]\n"
  "v_exp_f32 v99, v99\n"
  "v_mfma_f32_16x16x32_bf16 v[148:151], a[148:151], v[64:67], v[148:151]\n"
  "s_xor_b32 s69, s69, 0x8000\n"
  "ds_read_b128 a[72:75], v10 offset:0\n"
  "v_mfma_f32_16x16x32_bf16 v[148:151], a[180:183], v[80:83], v[148:151]\n"
  "v_exp_f32 v100, v100\n"
  "v_mfma_f32_16x16x32_bf16 v[152:155], a[152:155], v[64:67], v[152:155]\n"
  "s_waitcnt lgkmcnt(3)\n"
  "ds_read_b128 a[76:79], v11 offset:0\n"
  "v_mfma_f32_16x16x32_bf16 v[152:155], a[184:187], v[80:83], v[152:155]\n"
  "v_exp_f32 v101, v101\n"
  "v_mfma_f32_16x16x32_bf16 v[156:159], a[156:159], v[64:67], v[156:159]\n"
  "ds_read_b128 a[80:83], v8 offset:4096\n"
  "v_cvt_pk_bf16_f32 v96, v96, v97\n"
  "v_mfma_f32_16x16x32_bf16 v[156:159], a[188:191], v[80:83], v[156:159]\n"
  "v_exp_f32 v102, v102\n"
  "v_mfma_f32_16x16x32_bf16 v[48:51], a[192:195], v[64:67], v[48:51]\n"
  "ds_read_b128 a[84:87], v9 offset:4096\n"
  "v_cvt_pk_bf16_f32 v97, v98, v99\n"
  "v_mfma_f32_16x16x32_bf16 v[48:51], a[192:195], v[80:83], v[48:51]\n"
  "buffer_load_dwordx4 v16, s[44:47], s67 offen offset:2048 nt lds\n"
  "v_mfma_f32_16x16x32_bf16 v[160:163], a[128:131], v[72:75], v[160:163]\n"
  "v_exp_f32 v103, v103\n"
  "v_mfma_f32_16x16x32_bf16 v[160:163], a[160:163], v[88:91], v[160:163]\n"
  "ds_read_b128 a[88:91], v10 offset:4096\n"
  "v_cvt_pk_bf16_f32 v98, v100, v101\n"
  "v_mfma_f32_16x16x32_bf16 v[164:167], a[132:135], v[72:75], v[164:167]\n"
  "v_exp_f32 v112, v112\n"
  "v_mfma_f32_16x16x32_bf16 v[164:167], a[164:167], v[88:91], v[164:167]\n"
  "ds_read_b128 a[92:95], v11 offset:4096\n"
  "v_cvt_pk_bf16_f32 v99, v102, v103\n"
  "v_mfma_f32_16x16x32_bf16 v[168:171], a[136:139], v[72:75], v[168:171]\n"
  "v_exp_f32 v113, v113\n"
  "v_mfma_f32_16x16x32_bf16 v[168:171], a[168:171], v[88:91], v[168:171]\n"
  "ds_read_b128 a[96:99], v8 offset:8192\n"
  "v_cvt_pk_bf16_f32 v112, v112, v113\n"
  "v_mfma_f32_16x16x32_bf16 v[172:175], a[140:143], v[72:75], v[172:175]\n"
  "v_exp_f32 v114, v114\n"
  "v_mfma_f32_16x16x32_bf16 v[172:175], a[172:175], v[88:91], v[172:175]\n"
  "v_exp_f32 v115, v115\n"
  "v_mfma_f32_16x16x32_bf16 v[176:179], a[144:147], v[72:75], v[176:179]\n"
  "ds_read_b128 a[100:103], v9 offset:8192\n"
  "v_cvt_pk_bf16_f32 v113, v114, v115\n"
  "v_mfma_f32_16x16x32_bf16 v[176:179], a[176:179], v[88:91], v[176:179]\n"
  "v_exp_f32 v116, v116\n"
  "v_mfma_f32_16x16x32_bf16 v[180:183], a[148:151], v[72:75], v[180:183]\n"
  "ds_read_b128 a[104:107], v10 offset:8192\n"
  "v_mfma_f32_16x16x32_bf16 v[180:183], a[180:183], v[88:91], v[180:183]\n"
  "buffer_load_dwordx4 v17, s[44:47], s67 offen offset:3072 nt lds\n"
  "v_mfma_f32_16x16x32_bf16 v[184:187], a[152:155], v[72:75], v[184:187]\n"
  "v_exp_f32 v117, v117\n"
  "v_mfma_f32_16x16x32_bf16 v[184:187], a[184:187], v[88:91], v[184:187]\n"
  "ds_read_b128 a[108:111], v11 offset:8192\n"
  "v_cvt_pk_bf16_f32 v114, v116, v117\n"
  "v_mfma_f32_16x16x32_bf16 v[188:191], a[156:159], v[72:75], v[188:191]\n"
  "v_exp_f32 v118, v118\n"
  "v_mfma_f32_16x16x32_bf16 v[188:191], a[188:191], v[88:91], v[188:191]\n"
  "ds_read_b128 a[112:115], v8 offset:12288\n"
  "v_mfma_f32_16x16x32_bf16 v[52:55], a[192:195], v[72:75], v[52:55]\n"
  "v_exp_f32 v119, v119\n"
  "v_mfma_f32_16x16x32_bf16 v[52:55], a[192:195], v[88:91], v[52:55]\n"
  "s_waitcnt lgkmcnt(2)\n"
  "ds_read_b128 a[116:119], v9 offset:12288\n"
  "v_mfma_f32_16x16x32_bf16 v[64:67], a[64:67], a[0:3], v[32:35]\n"
  "v_exp_f32 v104, v104\n"
  "v_mfma_f32_16x16x32_bf16 v[64:67], a[68:71], a[4:7], v[64:67]\n"
  "ds_read_b128 a[120:123], v10 offset:12288\n"
  "v_cvt_pk_bf16_f32 v115, v118, v119\n"
  "v_mfma_f32_16x16x32_bf16 v[64:67], a[72:75], a[8:11], v[64:67]\n"
  "v_exp_f32 v105, v105\n"
  "v_mfma_f32_16x16x32_bf16 v[64:67], a[76:79], a[12:15], v[64:67]\n"
  "ds_read_b128 a[124:127], v11 offset:12288\n"
  "v_cvt_pk_bf16_f32 v104, v104, v105\n"
  "v_mfma_f32_16x16x32_bf16 v[72:75], a[64:67], a[16:19], v[36:39]\n"
  "v_exp_f32 v106, v106\n"
  "v_mfma_f32_16x16x32_bf16 v[72:75], a[68:71], a[20:23], v[72:75]\n"
  "v_xor_b32 v8, s69, v8\n"
  "v_mfma_f32_16x16x32_bf16 v[72:75], a[72:75], a[24:27], v[72:75]\n"
  "v_exp_f32 v107, v107\n"
  "v_mfma_f32_16x16x32_bf16 v[72:75], a[76:79], a[28:31], v[72:75]\n"
  "v_xor_b32 v9, s69, v9\n"
  "v_cvt_pk_bf16_f32 v105, v106, v107\n"
  "v_mfma_f32_16x16x32_bf16 v[68:71], a[80:83], a[0:3], v[32:35]\n"
  "v_exp_f32 v108, v108\n"
  "v_mfma_f32_16x16x32_bf16 v[68:71], a[84:87], a[4:7], v[68:71]\n"
  "v_xor_b32 v10, s69, v10\n"
  "v_mfma_f32_16x16x32_bf16 v[68:71], a[88:91], a[8:11], v[68:71]\n"
  "v_exp_f32 v109, v109\n"
  "v_mfma_f32_16x16x32_bf16 v[68:71], a[92:95], a[12:15], v[68:71]\n"
  "v_xor_b32 v11, s69, v11\n"
  "v_cvt_pk_bf16_f32 v106, v108, v109\n"
  "v_mfma_f32_16x16x32_bf16 v[76:79], a[80:83], a[16:19], v[36:39]\n"
  "v_exp_f32 v110, v110\n"
  "v_mfma_f32_16x16x32_bf16 v[76:79], a[84:87], a[20:23], v[76:79]\n"
  "v_mfma_f32_16x16x32_bf16 v[76:79], a[88:91], a[24:27], v[76:79]\n"
  "v_exp_f32 v111, v111\n"
  "v_mfma_f32_16x16x32_bf16 v[76:79], a[92:95], a[28:31], v[76:79]\n"
  "v_cvt_pk_bf16_f32 v107, v110, v111\n"
  "v_mfma_f32_16x16x32_bf16 v[80:83], a[96:99], a[0:3], v[32:35]\n"
  "v_exp_f32 v120, v120\n"
  "v_mfma_f32_16x16x32_bf16 v[80:83], a[100:103], a[4:7], v[80:83]\n"
  "s_waitcnt lgkmcnt(0)\n"
  "v_mfma_f32_16x16x32_bf16 v[80:83], a[104:107], a[8:11], v[80:83]\n"
  "v_exp_f32 v121, v121\n"
  "v_mfma_f32_16x16x32_bf16 v[80:83], a[108:111], a[12:15], v[80:83]\n"
  "v_cvt_pk_bf16_f32 v120, v120, v121\n"
  "v_mfma_f32_16x16x32_bf16 v[88:91], a[96:99], a[16:19], v[36:39]\n"
  "v_exp_f32 v122, v122\n"
  "v_mfma_f32_16x16x32_bf16 v[88:91], a[100:103], a[20:23], v[88:91]\n"
  "v_mfma_f32_16x16x32_bf16 v[88:91], a[104:107], a[24:27], v[88:91]\n"
  "v_exp_f32 v123, v123\n"
  "v_mfma_f32_16x16x32_bf16 v[88:91], a[108:111], a[28:31], v[88:91]\n"
  "v_cvt_pk_bf16_f32 v121, v122, v123\n"
  "v_mfma_f32_16x16x32_bf16 v[84:87], a[112:115], a[0:3], v[32:35]\n"
  "v_exp_f32 v124, v124\n"
  "v_mfma_f32_16x16x32_bf16 v[84:87], a[116:119], a[4:7], v[84:87]\n"
  "v_mfma_f32_16x16x32_bf16 v[84:87], a[120:123], a[8:11], v[84:87]\n"
  "v_exp_f32 v125, v125\n"
  "v_mfma_f32_16x16x32_bf16 v[84:87], a[124:127], a[12:15], v[84:87]\n"
  "v_cvt_pk_bf16_f32 v122, v124, v125\n"
  "v_mfma_f32_16x16x32_bf16 v[92:95], a[112:115], a[16:19], v[36:39]\n"
  "v_exp_f32 v126, v126\n"
  "v_mfma_f32_16x16x32_bf16 v[92:95], a[116:119], a[20:23], v[92:95]\n"
  "v_mfma_f32_16x16x32_bf16 v[92:95], a[120:123], a[24:27], v[92:95]\n"
  "v_exp_f32 v127, v127\n"
  "v_mfma_f32_16x16x32_bf16 v[92:95], a[124:127], a[28:31], v[92:95]\n"
  "v_cvt_pk_bf16_f32 v123, v126, v127\n"
  "s_cmp_eq_u32 s65, s68\n"
  "v_mfma_f32_16x16x32_bf16 v[192:195], a[128:131], v[96:99], v[192:195]\n"
  "s_waitcnt vmcnt(8) lgkmcnt(0)\n"
  "s_barrier\n"
  "v_mfma_f32_16x16x32_bf16 v[192:195], a[160:163], v[112:115], v[192:195]\n"
  "s_cbranch_scc1 Lsm0%=\n"
  "Lbm0%=:\n"
  "v_mfma_f32_16x16x32_bf16 v[196:199], a[132:135], v[96:99], v[196:199]\n"
  "v_exp_f32 v64, v64\n"
  "v_mfma_f32_16x16x32_bf16 v[196:199], a[164:167], v[112:115], v[196:199]\n"
  "v_exp_f32 v65, v65\n"
  "v_mfma_f32_16x16x32_bf16 v[200:203], a[136:139], v[96:99], v[200:203]\n"
  "s_bitset1_b32 m0, 16\n"
  "v_cvt_pk_bf16_f32 v64, v64, v65\n"
  "v_mfma_f32_16x16x32_bf16 v[200:203], a[168:171], v[112:115], v[200:203]\n"
  "buffer_load_dwordx4 v18, s[48:51], s67 offen offset:0 nt lds\n"
  "v_mfma_f32_16x16x32_bf16 v[204:207], a[140:143], v[96:99], v[204:207]\n"
  "v_exp_f32 v66, v66\n"
  "v_mfma_f32_16x16x32_bf16 v[204:207], a[172:175], v[112:115], v[204:207]\n"
  "v_mfma_f32_16x16x32_bf16 v[208:211], a[144:147], v[96:99], v[208:211]\n"
  "v_exp_f32 v67, v67\n"
  "v_mfma_f32_16x16x32_bf16 v[208:211], a[176:179], v[112:115], v[208:211]\n"
  "v_cvt_pk_bf16_f32 v65, v66, v67\n"
  "v_mfma_f32_16x16x32_bf16 v[212:215], a[148:151], v[96:99], v[212:215]\n"
  "v_exp_f32 v68, v68\n"
  "v_mfma_f32_16x16x32_bf16 v[212:215], a[180:183], v[112:115], v[212:215]\n"
  "v_mfma_f32_16x16x32_bf16 v[216:219], a[152:155], v[96:99], v[216:219]\n"
  "v_exp_f32 v69, v69\n"
  "v_mfma_f32_16x16x32_bf16 v[216:219], a[184:187], v[112:115], v[216:219]\n"
  "v_cvt_pk_bf16_f32 v66, v68, v69\n"
  "v_mfma_f32_16x16x32_bf16 v[220:223], a[156:159], v[96:99], v[220:223]\n"
  "v_exp_f32 v70, v70\n"
  "v_mfma_f32_16x16x32_bf16 v[220:223], a[188:191], v[112:115], v[220:223]\n"
  "v_mfma_f32_16x16x32_bf16 v[56:59], a[192:195], v[96:99], v[56:59]\n"
  "v_exp_f32 v71, v71\n"
  "v_mfma_f32_16x16x32_bf16 v[56:59], a[192:195], v[112:115], v[56:59]\n"
  "buffer_load_dwordx4 v19, s[48:51], s67 offen offset:1024 nt lds\n"
  "v_mfma_f32_16x16x32_bf16 v[224:227], a[128:131], v[104:107], v[224:227]\n"
  "ds_read_b64_tr_b16 a[128:129], v12 offset:0\n"
  "ds_read_b64_tr_b16 a[130:131], v12 offset:4096\n"
  "v_mfma_f32_16x16x32_bf16 v[224:227], a[160:163], v[120:123], v[224:227]\n"
  "v_exp_f32 v80, v80\n"
  "v_mfma_f32_16x16x32_bf16 v[228:231], a[132:135], v[104:107], v[228:231]\n"
  "ds_read_b64_tr_b16 a[160:161], v12 offset:8192\n"
  "ds_read_b64_tr_b16 a[162:163], v12 offset:12288\n"
  "v_mfma_f32_16x16x32_bf16 v[228:231], a[164:167], v[120:123], v[228:231]\n"
  "v_exp_f32 v81, v81\n"
  "v_mfma_f32_16x16x32_bf16 v[232:235], a[136:139], v[104:107], v[232:235]\n"
  "ds_read_b64_tr_b16 a[132:133], v13 offset:0\n"
  "ds_read_b64_tr_b16 a[134:135], v13 offset:4096\n"
  "v_mfma_f32_16x16x32_bf16 v[232:235], a[168:171], v[120:123], v[232:235]\n"
  "v_exp_f32 v82, v82\n"
  "v_mfma_f32_16x16x32_bf16 v[236:239], a[140:143], v[104:107], v[236:239]\n"
  "ds_read_b64_tr_b16 a[164:165], v13 offset:8192\n"
  "ds_read_b64_tr_b16 a[166:167], v13 offset:12288\n"
  "v_mfma_f32_16x16x32_bf16 v[236:239], a[172:175], v[120:123], v[236:239]\n"
  "v_exp_f32 v83, v83\n"
  "v_mfma_f32_16x16x32_bf16 v[240:243], a[144:147], v[104:107], v[240:243]\n"
  "ds_read_b64_tr_b16 a[136:137], v12 offset:256\n"
  "ds_read_b64_tr_b16 a[138:139], v12 offset:4352\n"
  "v_mfma_f32_16x16x32_bf16 v[240:243], a[176:179], v[120:123], v[240:243]\n"
  "v_exp_f32 v84, v84\n"
  "v_mfma_f32_16x16x32_bf16 v[244:247], a[148:151], v[104:107], v[244:247]\n"
  "ds_read_b64_tr_b16 a[168:169], v12 offset:8448\n"
  "ds_read_b64_tr_b16 a[170:171], v12 offset:12544\n"
  "v_mfma_f32_16x16x32_bf16 v[244:247], a[180:183], v[120:123], v[244:247]\n"
  "buffer_load_dwordx4 v18, s[48:51], s67 offen offset:2048 nt lds\n"
  "v_mfma_f32_16x16x32_bf16 v[248:251], a[152:155], v[104:107], v[248:251]\n"
  "v_exp_f32 v85, v85\n"
  "v_mfma_f32_16x16x32_bf16 v[248:251], a[184:187], v[120:123], v[248:251]\n"
  "ds_read_b64_tr_b16 a[140:141], v13 offset:256\n"
  "ds_read_b64_tr_b16 a[142:143], v13 offset:4352\n"
  "v_mfma_f32_16x16x32_bf16 v[252:255], a[156:159], v[104:107], v[252:255]\n"
  "v_exp_f32 v86, v86\n"
  "v_mfma_f32_16x16x32_bf16 v[252:255], a[188:191], v[120:123], v[252:255]\n"
  "ds_read_b64_tr_b16 a[172:173], v13 offset:8448\n"
  "ds_read_b64_tr_b16 a[174:175], v13 offset:12544\n"
  "v_mfma_f32_16x16x32_bf16 v[60:63], a[192:195], v[104:107], v[60:63]\n"
  "v_exp_f32 v87, v87\n"
  "v_mfma_f32_16x16x32_bf16 v[60:63], a[192:195], v[120:123], v[60:63]\n"
  "ds_read_b64_tr_b16 a[144:145], v12 offset:512\n"
  "ds_read_b64_tr_b16 a[146:147], v12 offset:4608\n"
  "v_mfma_f32_16x16x32_bf16 v[96:99], a[64:67], a[32:35], v[40:43]\n"
  "v_exp_f32 v72, v72\n"
  "v_mfma_f32_16x16x32_bf16 v[96:99], a[68:71], a[36:39], v[96:99]\n"
  "ds_read_b64_tr_b16 a[176:177], v12 offset:8704\n"
  "ds_read_b64_tr_b16 a[178:179], v12 offset:12800\n"
  "v_mfma_f32_16x16x32_bf16 v[96:99], a[72:75], a[40:43], v[96:99]\n"
  "v_exp_f32 v73, v73\n"
  "v_mfma_f32_16x16x32_bf16 v[96:99], a[76:79], a[44:47], v[96:99]\n"
  "ds_read_b64_tr_b16 a[148:149], v13 offset:512\n"
  "v_cvt_pk_bf16_f32 v67, v70, v71\n"
  "v_mfma_f32_16x16x32_bf16 v[104:107], a[64:67], a[48:51], v[44:47]\n"
  "v_exp_f32 v74, v74\n"
  "v_mfma_f32_16x16x32_bf16 v[104:107], a[68:71], a[52:55], v[104:107]\n"
  "buffer_load_dwordx4 v19, s[48:51], s67 offen offset:3072 nt lds\n"
  "v_mfma_f32_16x16x32_bf16 v[104:107], a[72:75], a[56:59], v[104:107]\n"
  "s_add_u32 s66, s66, 0x4000\n"
  "v_readlane_b32 s67, v20, s65\n"
  "v_mfma_f32_16x16x32_bf16 v[104:107], a[76:79], a[60:63], v[104:107]\n"
  "v_exp_f32 v75, v75\n"
  "v_mfma_f32_16x16x32_bf16 v[100:103], a[80:83], a[32:35], v[40:43]\n"
  "s_and_b32 m0, s66, 0xffff\n"
  "s_add_u32 s65, s65, 1\n"
  "v_mfma_f32_16x16x32_bf16 v[100:103], a[84:87], a[36:39], v[100:103]\n"
  "v_exp_f32 v76, v76\n"
  "v_mfma_f32_16x16x32_bf16 v[100:103], a[88:91], a[40:43], v[100:103]\n"
  "ds_read_b64_tr_b16 a[150:151], v13 offset:4608\n"
  "v_cvt_pk_bf16_f32 v80, v80, v81\n"
  "v_mfma_f32_16x16x32_bf16 v[100:103], a[92:95], a[44:47], v[100:103]\n"
  "v_exp_f32 v77, v77\n"
  "v_mfma_f32_16x16x32_bf16 v[108:111], a[80:83], a[48:51], v[44:47]\n"
  "v_exp_f32 v78, v78\n"
  "v_mfma_f32_16x16x32_bf16 v[108:111], a[84:87], a[52:55], v[108:111]\n"
  "ds_read_b64_tr_b16 a[180:181], v13 offset:8704\n"
  "v_cvt_pk_bf16_f32 v81, v82, v83\n"
  "v_mfma_f32_16x16x32_bf16 v[108:111], a[88:91], a[56:59], v[108:111]\n"
  "v_exp_f32 v79, v79\n"
  "v_mfma_f32_16x16x32_bf16 v[108:111], a[92:95], a[60:63], v[108:111]\n"
  "ds_read_b64_tr_b16 a[182:183], v13 offset:12800\n"
  "v_cvt_pk_bf16_f32 v82, v84, v85\n"
  "v_mfma_f32_16x16x32_bf16 v[112:115], a[96:99], a[32:35], v[40:43]\n"
  "v_exp_f32 v88, v88\n"
  "v_mfma_f32_16x16x32_bf16 v[112:115], a[100:103], a[36:39], v[112:115]\n"
  "ds_read_b64_tr_b16 a[152:153], v12 offset:768\n"
  "v_cvt_pk_bf16_f32 v83, v86, v87\n"
  "v_mfma_f32_16x16x32_bf16 v[112:115], a[104:107], a[40:43], v[112:115]\n"
  "buffer_load_dwordx4 v14, s[44:47], s67 offen offset:0 nt lds\n"
  "v_mfma_f32_16x16x32_bf16 v[112:115], a[108:111], a[44:47], v[112:115]\n"
  "v_exp_f32 v89, v89\n"
  "v_mfma_f32_16x16x32_bf16 v[120:123], a[96:99], a[48:51], v[44:47]\n"
  "ds_read_b64_tr_b16 a[154:155], v12 offset:4864\n"
  "v_cvt_pk_bf16_f32 v72, v72, v73\n"
  "v_mfma_f32_16x16x32_bf16 v[120:123], a[100:103], a[52:55], v[120:123]\n"
  "v_exp_f32 v90, v90\n"
  "v_mfma_f32_16x16x32_bf16 v[120:123], a[104:107], a[56:59], v[120:123]\n"
  "ds_read_b64_tr_b16 a[184:185], v12 offset:8960\n"
  "v_cvt_pk_bf16_f32 v73, v74, v75\n"
  "v_mfma_f32_16x16x32_bf16 v[120:123], a[108:111], a[60:63], v[120:123]\n"
  "v_exp_f32 v91, v91\n"
  "v_mfma_f32_16x16x32_bf16 v[116:119], a[112:115], a[32:35], v[40:43]\n"
  "ds_read_b64_tr_b16 a[186:187], v12 offset:13056\n"
  "v_cvt_pk_bf16_f32 v74, v76, v77\n"
  "v_mfma_f32_16x16x32_bf16 v[116:119], a[116:119], a[36:39], v[116:119]\n"
  "v_exp_f32 v92, v92\n"
  "v_mfma_f32_16x16x32_bf16 v[116:119], a[120:123], a[40:43], v[116:119]\n"
  "ds_read_b64_tr_b16 a[156:157], v13 offset:768\n"
  "v_cvt_pk_bf16_f32 v75, v78, v79\n"
  "v_mfma_f32_16x16x32_bf16 v[116:119], a[124:127], a[44:47], v[116:119]\n"
  "v_exp_f32 v93, v93\n"
  "v_mfma_f32_16x16x32_bf16 v[124:127], a[112:115], a[48:51], v[44:47]\n"
  "ds_read_b64_tr_b16 a[158:159], v13 offset:4864\n"
  "v_cvt_pk_bf16_f32 v88, v88, v89\n"
  "v_mfma_f32_16x16x32_bf16 v[124:127], a[116:119], a[52:55], v[124:127]\n"
  "v_exp_f32 v94, v94\n"
  "v_mfma_f32_16x16x32_bf16 v[124:127], a[120:123], a[56:59], v[124:127]\n"
  "v_cvt_pk_bf16_f32 v89, v90, v91\n"
  "s_cmp_lt_u32 s65, s64\n"
  "v_mfma_f32_16x16x32_bf16 v[124:127], a[124:127], a[60:63], v[124:127]\n"
  "s_waitcnt lgkmcnt(2)\n"
  "s_cbranch_scc1 Lloop0%=\n"
  "Lfinal%=:\n"
  "v_mfma_f32_16x16x32_bf16 v[128:131], a[128:131], v[64:67], v[128:131]\n"
  "ds_read_b64_tr_b16 a[188:189], v13 offset:8960\n"
  "s_lshl_b32 s67, s95, 10\n"
  "v_mfma_f32_16x16x32_bf16 v[128:131], a[160:163], v[80:83], v[128:131]\n"
  "ds_read_b64_tr_b16 a[190:191], v13 offset:13056\n"
  "v_lshlrev_b32 v8, 3, v24\n"
  "v_mfma_f32_16x16x32_bf16 v[132:135], a[132:135], v[64:67], v[132:135]\n"
  "s_add_u32 s67, s67, 17301504\n"
  "buffer_load_dwordx2 v[8:9], v8, s[56:59], s67 offen offset:512 sc1\n"
  "v_mfma_f32_16x16x32_bf16 v[132:135], a[164:167], v[80:83], v[132:135]\n"
  "v_exp_f32 v95, v95\n"
  "v_mfma_f32_16x16x32_bf16 v[136:139], a[136:139], v[64:67], v[136:139]\n"
  "v_cvt_pk_bf16_f32 v90, v92, v93\n"
  "v_cvt_pk_bf16_f32 v91, v94, v95\n"
  "v_mfma_f32_16x16x32_bf16 v[136:139], a[168:171], v[80:83], v[136:139]\n"
  "s_cmp_lg_u32 s90, 0\n"
  "s_cbranch_scc1 Lsf%=\n"
  "Lbf%=:\n"
  "v_mfma_f32_16x16x32_bf16 v[140:143], a[140:143], v[64:67], v[140:143]\n"
  "v_cvt_pk_bf16_f32 v128, v128, v129\n"
  "v_cvt_pk_bf16_f32 v129, v130, v131\n"
  "v_mfma_f32_16x16x32_bf16 v[140:143], a[172:175], v[80:83], v[140:143]\n"
  "v_cvt_pk_bf16_f32 v130, v132, v133\n"
  "v_cvt_pk_bf16_f32 v131, v134, v135\n"
  "v_mfma_f32_16x16x32_bf16 v[144:147], a[144:147], v[64:67], v[144:147]\n"
  "buffer_store_dwordx4 v[128:131], v22, s[56:59], s76 offen offset:0 sc1\n"
  "v_mfma_f32_16x16x32_bf16 v[144:147], a[176:179], v[80:83], v[144:147]\n"
  "v_cvt_pk_bf16_f32 v136, v136, v137\n"
  "v_cvt_pk_bf16_f32 v137, v138, v139\n"
  "v_mfma_f32_16x16x32_bf16 v[148:151], a[148:151], v[64:67], v[148:151]\n"
  "v_cvt_pk_bf16_f32 v138, v140, v141\n"
  "v_cvt_pk_bf16_f32 v139, v142, v143\n"
  "v_mfma_f32_16x16x32_bf16 v[148:151], a[180:183], v[80:83], v[148:151]\n"
  "v_exp_f32 v96, v96\n"
  "v_mfma_f32_16x16x32_bf16 v[152:155], a[152:155], v[64:67], v[152:155]\n"
  "buffer_store_dwordx4 v[136:139], v22, s[56:59], s76 offen offset:1024 sc1\n"
  "v_mfma_f32_16x16x32_bf16 v[152:155], a[184:187], v[80:83], v[152:155]\n"
  "v_cvt_pk_bf16_f32 v144, v144, v145\n"
  "v_cvt_pk_bf16_f32 v145, v146, v147\n"
  "s_waitcnt lgkmcnt(0)\n"
  "v_mfma_f32_16x16x32_bf16 v[156:159], a[156:159], v[64:67], v[156:159]\n"
  "v_cvt_pk_bf16_f32 v146, v148, v149\n"
  "v_mfma_f32_16x16x32_bf16 v[156:159], a[188:191], v[80:83], v[156:159]\n"
  "v_cvt_pk_bf16_f32 v147, v150, v151\n"
  "v_exp_f32 v97, v97\n"
  "v_mfma_f32_16x16x32_bf16 v[160:163], a[128:131], v[72:75], v[160:163]\n"
  "buffer_store_dwordx4 v[144:147], v22, s[56:59], s76 offen offset:2048 sc1\n"
  "v_mfma_f32_16x16x32_bf16 v[160:163], a[160:163], v[88:91], v[160:163]\n"
  "v_cvt_pk_bf16_f32 v152, v152, v153\n"
  "v_cvt_pk_bf16_f32 v153, v154, v155\n"
  "v_mfma_f32_16x16x32_bf16 v[164:167], a[132:135], v[72:75], v[164:167]\n"
  "v_cvt_pk_bf16_f32 v154, v156, v157\n"
  "v_cvt_pk_bf16_f32 v155, v158, v159\n"
  "v_mfma_f32_16x16x32_bf16 v[164:167], a[164:167], v[88:91], v[164:167]\n"
  "v_exp_f32 v98, v98\n"
  "v_mfma_f32_16x16x32_bf16 v[168:171], a[136:139], v[72:75], v[168:171]\n"
  "buffer_store_dwordx4 v[152:155], v22, s[56:59], s76 offen offset:3072 sc1\n"
  "v_mfma_f32_16x16x32_bf16 v[168:171], a[168:171], v[88:91], v[168:171]\n"
  "v_cvt_pk_bf16_f32 v160, v160, v161\n"
  "v_cvt_pk_bf16_f32 v161, v162, v163\n"
  "v_mfma_f32_16x16x32_bf16 v[172:175], a[140:143], v[72:75], v[172:175]\n"
  "v_exp_f32 v99, v99\n"
  "v_mfma_f32_16x16x32_bf16 v[172:175], a[172:175], v[88:91], v[172:175]\n"
  "v_cvt_pk_bf16_f32 v162, v164, v165\n"
  "v_cvt_pk_bf16_f32 v163, v166, v167\n"
  "v_mfma_f32_16x16x32_bf16 v[176:179], a[144:147], v[72:75], v[176:179]\n"
  "buffer_store_dwordx4 v[160:163], v22, s[56:59], s77 offen offset:0 sc1\n"
  "v_mfma_f32_16x16x32_bf16 v[176:179], a[176:179], v[88:91], v[176:179]\n"
  "v_cvt_pk_bf16_f32 v168, v168, v169\n"
  "v_cvt_pk_bf16_f32 v169, v170, v171\n"
  "v_mfma_f32_16x16x32_bf16 v[180:183], a[148:151], v[72:75], v[180:183]\n"
  "v_cvt_pk_bf16_f32 v170, v172, v173\n"
  "v_cvt_pk_bf16_f32 v171, v174, v175\n"
  "v_mfma_f32_16x16x32_bf16 v[180:183], a[180:183], v[88:91], v[180:183]\n"
  "v_exp_f32 v100, v100\n"
  "v_mfma_f32_16x16x32_bf16 v[184:187], a[152:155], v[72:75], v[184:187]\n"
  "buffer_store_dwordx4 v[168:171], v22, s[56:59], s77 offen offset:1024 sc1\n"
  "v_mfma_f32_16x16x32_bf16 v[184:187], a[184:187], v[88:91], v[184:187]\n"
  "v_cvt_pk_bf16_f32 v176, v176, v177\n"
  "v_cvt_pk_bf16_f32 v177, v178, v179\n"
  "v_mfma_f32_16x16x32_bf16 v[188:191], a[156:159], v[72:75], v[188:191]\n"
  "v_exp_f32 v101, v101\n"
  "v_mfma_f32_16x16x32_bf16 v[188:191], a[188:191], v[88:91], v[188:191]\n"
  "v_cvt_pk_bf16_f32 v178, v180, v181\n"
  "v_cvt_pk_bf16_f32 v179, v182, v183\n"
  "v_mfma_f32_16x16x32_bf16 v[48:51], a[192:195], v[64:67], v[48:51]\n"
  "buffer_store_dwordx4 v[176:179], v22, s[56:59], s77 offen offset:2048 sc1\n"
  "v_mfma_f32_16x16x32_bf16 v[48:51], a[192:195], v[80:83], v[48:51]\n"
  "v_cvt_pk_bf16_f32 v184, v184, v185\n"
  "v_cvt_pk_bf16_f32 v185, v186, v187\n"
  "v_mfma_f32_16x16x32_bf16 v[52:55], a[192:195], v[72:75], v[52:55]\n"
  "v_cvt_pk_bf16_f32 v186, v188, v189\n"
  "v_cvt_pk_bf16_f32 v187, v190, v191\n"
  "v_mfma_f32_16x16x32_bf16 v[52:55], a[192:195], v[88:91], v[52:55]\n"
  "v_cvt_pk_bf16_f32 v96, v96, v97\n"
  "buffer_store_dwordx4 v[184:187], v22, s[56:59], s77 offen offset:3072 sc1\n"
  "v_exp_f32 v102, v102\n"
  "v_cvt_pk_bf16_f32 v97, v98, v99\n"
  "v_exp_f32 v103, v103\n"
  "v_cvt_pk_bf16_f32 v98, v100, v101\n"
  "v_exp_f32 v112, v112\n"
  "v_cvt_pk_bf16_f32 v99, v102, v103\n"
  "v_exp_f32 v113, v113\n"
  "v_exp_f32 v114, v114\n"
  "v_mfma_f32_16x16x32_bf16 v[56:59], a[192:195], v[96:99], v[56:59]\n"
  "v_cvt_pk_bf16_f32 v112, v112, v113\n"
  "v_exp_f32 v115, v115\n"
  "v_exp_f32 v116, v116\n"
  "v_cvt_pk_bf16_f32 v113, v114, v115\n"
  "v_exp_f32 v117, v117\n"
  "v_exp_f32 v118, v118\n"
  "v_cvt_pk_bf16_f32 v114, v116, v117\n"
  "v_exp_f32 v119, v119\n"
  "v_exp_f32 v104, v104\n"
  "v_cvt_pk_bf16_f32 v115, v118, v119\n"
  "v_exp_f32 v105, v105\n"
  "v_exp_f32 v106, v106\n"
  "v_mfma_f32_16x16x32_bf16 v[56:59], a[192:195], v[112:115], v[56:59]\n"
  "v_cvt_pk_bf16_f32 v104, v104, v105\n"
  "v_exp_f32 v107, v107\n"
  "v_mfma_f32_16x16x32_bf16 v[192:195], a[128:131], v[96:99], v[192:195]\n"
  "v_cvt_pk_bf16_f32 v105, v106, v107\n"
  "v_exp_f32 v108, v108\n"
  "v_mfma_f32_16x16x32_bf16 v[192:195], a[160:163], v[112:115], v[192:195]\n"
  "v_exp_f32 v109, v109\n"
  "v_mfma_f32_16x16x32_bf16 v[196:199], a[132:135], v[96:99], v[196:199]\n"
  "v_cvt_pk_bf16_f32 v106, v108, v109\n"
  "v_exp_f32 v110, v110\n"
  "v_mfma_f32_16x16x32_bf16 v[196:199], a[164:167], v[112:115], v[196:199]\n"
  "v_exp_f32 v111, v111\n"
  "v_mfma_f32_16x16x32_bf16 v[200:203], a[136:139], v[96:99], v[200:203]\n"
  "v_cvt_pk_bf16_f32 v107, v110, v111\n"
  "v_exp_f32 v120, v120\n"
  "v_mfma_f32_16x16x32_bf16 v[200:203], a[168:171], v[112:115], v[200:203]\n"
  "v_exp_f32 v121, v121\n"
  "v_mfma_f32_16x16x32_bf16 v[204:207], a[140:143], v[96:99], v[204:207]\n"
  "v_cvt_pk_bf16_f32 v120, v120, v121\n"
  "v_exp_f32 v122, v122\n"
  "v_mfma_f32_16x16x32_bf16 v[204:207], a[172:175], v[112:115], v[204:207]\n"
  "v_exp_f32 v123, v123\n"
  "v_mfma_f32_16x16x32_bf16 v[208:211], a[144:147], v[96:99], v[208:211]\n"
  "v_cvt_pk_bf16_f32 v121, v122, v123\n"
  "v_exp_f32 v124, v124\n"
  "v_mfma_f32_16x16x32_bf16 v[208:211], a[176:179], v[112:115], v[208:211]\n"
  "v_exp_f32 v125, v125\n"
  "v_mfma_f32_16x16x32_bf16 v[212:215], a[148:151], v[96:99], v[212:215]\n"
  "v_cvt_pk_bf16_f32 v122, v124, v125\n"
  "v_exp_f32 v126, v126\n"
  "v_mfma_f32_16x16x32_bf16 v[212:215], a[180:183], v[112:115], v[212:215]\n"
  "v_exp_f32 v127, v127\n"
  "v_mfma_f32_16x16x32_bf16 v[216:219], a[152:155], v[96:99], v[216:219]\n"
  "v_cvt_pk_bf16_f32 v123, v126, v127\n"
  "v_permlane16_swap_b32 v32, v36\n"
  "v_mfma_f32_16x16x32_bf16 v[216:219], a[184:187], v[112:115], v[216:219]\n"
  "v_permlane16_swap_b32 v40, v44\n"
  "v_permlane16_swap_b32 v49, v53\n"
  "v_mfma_f32_16x16x32_bf16 v[220:223], a[156:159], v[96:99], v[220:223]\n"
  "v_permlane32_swap_b32 v32, v40\n"
  "v_sub_f32 v48, 0, v32\n"
  "v_mfma_f32_16x16x32_bf16 v[220:223], a[188:191], v[112:115], v[220:223]\n"
  "s_mov_b32 s98, 0x53800000\n"
  "s_lshl2_add_u32 s99, s92, 147456\n"
  "v_mfma_f32_16x16x32_bf16 v[60:63], a[192:195], v[104:107], v[60:63]\n"
  "v_mov_b32 v15, s99\n"
  "v_cvt_pk_bf16_f32 v192, v192, v193\n"
  "v_mfma_f32_16x16x32_bf16 v[60:63], a[192:195], v[120:123], v[60:63]\n"
  "v_cvt_pk_bf16_f32 v193, v194, v195\n"
  "v_cvt_pk_bf16_f32 v194, v196, v197\n"
  "v_mfma_f32_16x16x32_bf16 v[224:227], a[128:131], v[104:107], v[224:227]\n"
  "v_cvt_pk_bf16_f32 v195, v198, v199\n"
  "v_cvt_pk_bf16_f32 v200, v200, v201\n"
  "v_mfma_f32_16x16x32_bf16 v[224:227], a[160:163], v[120:123], v[224:227]\n"
  "v_cvt_pk_bf16_f32 v201, v202, v203\n"
  "v_cvt_pk_bf16_f32 v202, v204, v205\n"
  "v_mfma_f32_16x16x32_bf16 v[228:231], a[132:135], v[104:107], v[228:231]\n"
  "v_permlane16_swap_b32 v57, v61\n"
  "v_cvt_pk_bf16_f32 v203, v206, v207\n"
  "v_mfma_f32_16x16x32_bf16 v[228:231], a[164:167], v[120:123], v[228:231]\n"
  "v_permlane32_swap_b32 v49, v57\n"
  "v_cmp_nge_f32 vcc, s98, v49\n"
  "v_mfma_f32_16x16x32_bf16 v[232:235], a[136:139], v[104:107], v[232:235]\n"
  "buffer_store_dwordx2 v[48:49], v23, s[56:59], s76 offen sc1\n"
  "v_mfma_f32_16x16x32_bf16 v[232:235], a[168:171], v[120:123], v[232:235]\n"
  "s_or_b32 s12, vcc_lo, vcc_hi\n"
  "s_cmp_eq_u32 s11, 0\n"
  "s_cselect_b32 s12, s12, 0\n"
  "v_mfma_f32_16x16x32_bf16 v[236:239], a[140:143], v[104:107], v[236:239]\n"
  "v_mov_b32 v14, s12\n"
  "v_cvt_pk_bf16_f32 v208, v208, v209\n"
  "v_mfma_f32_16x16x32_bf16 v[236:239], a[172:175], v[120:123], v[236:239]\n"
  "ds_write_b32 v15, v14\n"
  "v_cvt_pk_bf16_f32 v209, v210, v211\n"
  "v_mfma_f32_16x16x32_bf16 v[240:243], a[144:147], v[104:107], v[240:243]\n"
  "buffer_store_dwordx4 v[192:195], v22, s[56:59], s78 offen offset:0 sc1\n"
  "v_mfma_f32_16x16x32_bf16 v[240:243], a[176:179], v[120:123], v[240:243]\n"
  "v_cvt_pk_bf16_f32 v210, v212, v213\n"
  "v_cvt_pk_bf16_f32 v211, v214, v215\n"
  "v_mfma_f32_16x16x32_bf16 v[244:247], a[148:151], v[104:107], v[244:247]\n"
  "v_cvt_pk_bf16_f32 v216, v216, v217\n"
  "v_cvt_pk_bf16_f32 v217, v218, v219\n"
  "v_mfma_f32_16x16x32_bf16 v[244:247], a[180:183], v[120:123], v[244:247]\n"
  "v_cvt_pk_bf16_f32 v218, v220, v221\n"
  "v_cvt_pk_bf16_f32 v219, v222, v223\n"
  "v_mfma_f32_16x16x32_bf16 v[248:251], a[152:155], v[104:107], v[248:251]\n"
  "buffer_store_dwordx4 v[200:203], v22, s[56:59], s78 offen offset:1024 sc1\n"
  "v_mfma_f32_16x16x32_bf16 v[248:251], a[184:187], v[120:123], v[248:251]\n"
  "v_cvt_pk_bf16_f32 v224, v224, v225\n"
  "v_cvt_pk_bf16_f32 v225, v226, v227\n"
  "v_mfma_f32_16x16x32_bf16 v[252:255], a[156:159], v[104:107], v[252:255]\n"
  "v_cvt_pk_bf16_f32 v226, v228, v229\n"
  "v_cvt_pk_bf16_f32 v227, v230, v231\n"
  "v_mfma_f32_16x16x32_bf16 v[252:255], a[188:191], v[120:123], v[252:255]\n"
  "v_cvt_pk_bf16_f32 v232, v232, v233\n"
  "buffer_store_dwordx4 v[208:211], v22, s[56:59], s78 offen offset:2048 sc1\n"
  "v_cvt_pk_bf16_f32 v233, v234, v235\n"
  "v_cvt_pk_bf16_f32 v234, v236, v237\n"
  "v_cvt_pk_bf16_f32 v235, v238, v239\n"
  "v_cvt_pk_bf16_f32 v240, v240, v241\n"
  "v_cvt_pk_bf16_f32 v241, v242, v243\n"
  "v_cvt_pk_bf16_f32 v242, v244, v245\n"
  "v_cvt_pk_bf16_f32 v243, v246, v247\n"
  "v_cvt_pk_bf16_f32 v248, v248, v249\n"
  "v_cvt_pk_bf16_f32 v249, v250, v251\n"
  "v_cvt_pk_bf16_f32 v250, v252, v253\n"
  "v_cvt_pk_bf16_f32 v251, v254, v255\n"
  "buffer_store_dwordx4 v[216:219], v22, s[56:59], s78 offen offset:3072 sc1\n"
  "buffer_store_dwordx4 v[224:227], v22, s[56:59], s79 offen offset:0 sc1\n"
  "buffer_store_dwordx4 v[232:235], v22, s[56:59], s79 offen offset:1024 sc1\n"
  "buffer_store_dwordx4 v[240:243], v22, s[56:59], s79 offen offset:2048 sc1\n"
  "buffer_store_dwordx4 v[248:251], v22, s[56:59], s79 offen offset:3072 sc1\n"
  "Lprep%=:\n"
  "v_add_u32 v26, 1, v26\n"
  "v_mov_b32 v14, 147456\n"
  "s_waitcnt lgkmcnt(0)\n"
  "s_barrier\n"
  "ds_read_b128 v[16:19], v14\n"
  "s_mov_b32 s21, 67584\n"
  "s_mov_b32 s22, 135168\n"
  "s_mov_b32 s23, 202752\n"
  "s_mov_b32 s24, 270336\n"
  "s_mov_b32 s25, 337920\n"
  "s_mov_b32 s26, 405504\n"
  "s_mov_b32 s27, 473088\n"
  "s_mov_b32 s28, 540672\n"
  "s_mov_b32 s29, 608256\n"
  "s_mov_b32 s30, 675840\n"
  "s_mov_b32 s31, 743424\n"
  "v_readfirstlane_b32 s38, v26\n"
  "v_readfirstlane_b32 s39, v27\n"
  "s_mov_b32 s89, 0x400000\n"
  "s_waitcnt vmcnt(17)\n"
  "Lhint%=:\n"
  "v_xor_b32 v9, s39, v9\n"
  "v_xor_b32 v8, s38, v8\n"
  "v_lshl_or_b32 v8, v9, 2, v8\n"
  "v_cmp_ne_u32 vcc, 0, v8\n"
  "v_cmp_gt_i32 s[68:69], 0, v9\n"
  "s_cbranch_vccnz Lhre%=\n"
  "Lhok%=:\n"
  "v_cmp_eq_u32 vcc, 0, v9\n"
  "s_bcnt1_i32_b64 s83, s[68:69]\n"
  "s_bcnt1_i32_b64 s98, vcc\n"
  "s_cmp_gt_u32 s83, s98\n"
  "s_cselect_b32 s86, 0x2000, 0\n"
  "s_add_u32 s86, s86, 17317888\n"
  "s_lshl_b32 s98, s95, 6\n"
  "s_add_u32 s98, s98, s18\n"
  "s_mul_i32 s98, s98, 67584\n"
  "s_and_b32 s99, s19, 7\n"
  "s_lshl2_add_u32 s83, s99, s92\n"
  "s_lshl_b32 s83, s83, 11\n"
  "s_add_u32 s83, s83, s98\n"
  "v_lshl_add_u32 v8, v24, 4, s83\n"
  "s_lshr_b32 s82, s92, 1\n"
  "s_lshl1_add_u32 s82, s99, s82\n"
  "s_lshl_b32 s83, s82, 7\n"
  "s_add_u32 s83, s83, s98\n"
  "s_add_u32 s83, s83, 65536\n"
  "v_and_b32 v15, 15, v24\n"
  "v_lshl_add_u32 v9, v15, 3, s83\n"
  "s_lshr_b32 s83, s19, 3\n"
  "s_lshl2_add_u32 s83, s83, s95\n"
  "s_lshl4_add_u32 s83, s83, s82\n"
  "s_lshl_b32 s83, s83, 5\n"
  "s_and_b32 s98, s92, 1\n"
  "s_add_u32 s83, s83, s98\n"
  "s_lshl_b32 s83, s83, 7\n"
  "s_lshl_b32 s98, s92, 6\n"
  "v_lshl_add_u32 v10, v15, 8, s83\n"
  "v_lshrrev_b32 v15, 4, v24\n"
  "v_lshlrev_b32 v11, 3, v24\n"
  "v_lshl_add_u32 v10, v15, 4, v10\n"
  "s_lshl_b32 s64, s95, 11\n"
  "s_add_u32 s64, s64, s86\n"
  "s_lshr_b32 s99, s99, 1\n"
  "s_lshl_b32 s99, s99, 9\n"
  "s_add_u32 s88, s64, s99\n"
  "s_add_u32 s98, s98, s94\n"
  "s_lshl_b32 s98, s98, 3\n"
  "s_add_u32 s64, s98, s64\n"
  "s_waitcnt lgkmcnt(0)\n"
  "v_or3_b32 v16, v16, v17, v18\n"
  "v_or_b32 v16, v16, v19\n"
  "s_bfm_b64 s[36:37], s17, s18\n"
  "v_cmp_ne_u32 vcc, 0, v16\n"
  "s_mov_b32 s12, 1\n"
  "s_mov_b32 s82, 0xffff0000\n"
  "s_cbranch_vccnz Lend%=\n"
  "v_mov_b32 v14, 0xff800000\n"
  "v_mov_b32 v15, 0\n"
  "v_mov_b64 v[32:33], 0\n"
  "v_mov_b64 v[34:35], 0\n"
  "v_mov_b64 v[36:37], 0\n"
  "v_mov_b64 v[38:39], 0\n"
  "v_mov_b64 v[40:41], 0\n"
  "v_mov_b64 v[42:43], 0\n"
  "v_mov_b64 v[44:45], 0\n"
  "v_mov_b64 v[46:47], 0\n"
  "v_mov_b64 v[64:65], v[14:15]\n"
  "v_mov_b64 v[66:67], v[14:15]\n"
  "v_mov_b64 v[68:69], v[14:15]\n"
  "v_mov_b64 v[70:71], v[14:15]\n"
  "v_mov_b64 v[72:73], v[14:15]\n"
  "v_mov_b64 v[74:75], v[14:15]\n"
  "v_mov_b64 v[76:77], v[14:15]\n"
  "v_mov_b64 v[78:79], v[14:15]\n"
  "v_mov_b64 v[80:81], v[14:15]\n"
  "v_mov_b64 v[82:83], v[14:15]\n"
  "v_mov_b64 v[84:85], v[14:15]\n"
  "v_mov_b64 v[86:87], v[14:15]\n"
  "s_cmp_le_u32 s17, 12\n"
  "s_cselect_b32 s65, -1, 0\n"
  "s_min_u32 s98, s17, 12\n"
  "s_getpc_b64 s[74:75]\n"
  "Lpc%=:\n"
  "s_lshl_b32 s98, s98, 7\n"
  "s_sub_u32 s98, Lfin%=-Lpc%=, s98\n"
  "s_add_u32 s80, s74, s98\n"
  "s_addc_u32 s81, s75, 0\n"
  "s_add_u32 s74, s74, Lls0%=-Lpc%=\n"
  "s_addc_u32 s75, s75, 0\n"
  "s_lshl2_add_u32 s66, s19, s92\n"
  "s_mov_b64 exec, 1\n"
  "s_waitcnt vmcnt(0)\n"
  "buffer_store_dwordx2 v[26:27], off, s[56:59], s64 sc1\n"
  "s_mov_b64 exec, -1\n"
  "Lpoll%=:\n"
  "s_mov_b64 exec, s[36:37]\n"
  "buffer_load_dwordx2 v[12:13], v11, s[56:59], s88 offen sc1\n"
  "s_mov_b64 exec, -1\n"
  "s_sub_u32 s89, s89, 1\n"
  "s_cbranch_scc1 Lgive%=\n"
  "s_waitcnt vmcnt(0)\n"
  "Lproc%=:\n"
  "v_cmp_eq_u64 vcc, s[38:39], v[12:13]\n"
  "s_nop 0\n"
  "s_and_b64 s[68:69], s[36:37], vcc\n"
  "s_cbranch_scc0 Lpoll%=\n"
  "s_lshr_b64 s[68:69], s[68:69], s18\n"
  "s_andn2_b64 s[36:37], s[36:37], vcc\n"
  "s_and_b32 s68, s68, s65\n"
  "Lscan%=:\n"
  "s_ff1_i32_b32 s98, s68\n"
  "s_cmp_eq_u32 s68, 0\n"
  "s_mul_i32 s98, s98, Lls1%=-Lls0%=\n"
  "s_cbranch_scc1 Lnext%=\n"
  "s_add_u32 s70, s74, s98\n"
  "s_addc_u32 s71, s75, 0\n"
  "s_setpc_b64 s[70:71]\n"
  "Lls0%=:\n"
  "s_bitcmp1_b32 s68, 0\n"
  "s_cbranch_scc0 Lscan%=\n"
  "s_bitset0_b32 s68, 0\n"
  "buffer_load_dwordx2 v[64:65], v9, s[56:59], 0 offen sc1\n"
  "buffer_load_dwordx4 v[128:131], v8, s[56:59], 0 offen offset:0 sc1\n"
  "buffer_load_dwordx4 v[132:135], v8, s[56:59], 0 offen offset:1024 sc1\n"
  "Lls1%=:\n"
  "s_bitcmp1_b32 s68, 1\n"
  "s_cbranch_scc0 Lscan%=\n"
  "s_bitset0_b32 s68, 1\n"
  "buffer_load_dwordx2 v[66:67], v9, s[56:59], s21 offen sc1\n"
  "buffer_load_dwordx4 v[136:139], v8, s[56:59], s21 offen offset:0 sc1\n"
  "buffer_load_dwordx4 v[140:143], v8, s[56:59], s21 offen offset:1024 sc1\n"
  "Lls2%=:\n"
  "s_bitcmp1_b32 s68, 2\n"
  "s_cbranch_scc0 Lscan%=\n"
  "s_bitset0_b32 s68, 2\n"
  "buffer_load_dwordx2 v[68:69], v9, s[56:59], s22 offen sc1\n"
  "buffer_load_dwordx4 v[144:147], v8, s[56:59], s22 offen offset:0 sc1\n"
  "buffer_load_dwordx4 v[148:151], v8, s[56:59], s22 offen offset:1024 sc1\n"
  "Lls3%=:\n"
  "s_bitcmp1_b32 s68, 3\n"
  "s_cbranch_scc0 Lscan%=\n"
  "s_bitset0_b32 s68, 3\n"
  "buffer_load_dwordx2 v[70:71], v9, s[56:59], s23 offen sc1\n"
  "buffer_load_dwordx4 v[152:155], v8, s[56:59], s23 offen offset:0 sc1\n"
  "buffer_load_dwordx4 v[156:159], v8, s[56:59], s23 offen offset:1024 sc1\n"
  "Lls4%=:\n"
  "s_bitcmp1_b32 s68, 4\n"
  "s_cbranch_scc0 Lscan%=\n"
  "s_bitset0_b32 s68, 4\n"
  "buffer_load_dwordx2 v[72:73], v9, s[56:59], s24 offen sc1\n"
  "buffer_load_dwordx4 v[160:163], v8, s[56:59], s24 offen offset:0 sc1\n"
  "buffer_load_dwordx4 v[164:167], v8, s[56:59], s24 offen offset:1024 sc1\n"
  "Lls5%=:\n"
  "s_bitcmp1_b32 s68, 5\n"
  "s_cbranch_scc0 Lscan%=\n"
  "s_bitset0_b32 s68, 5\n"
  "buffer_load_dwordx2 v[74:75], v9, s[56:59], s25 offen sc1\n"
  "buffer_load_dwordx4 v[168:171], v8, s[56:59], s25 offen offset:0 sc1\n"
  "buffer_load_dwordx4 v[172:175], v8, s[56:59], s25 offen offset:1024 sc1\n"
  "Lls6%=:\n"
  "s_bitcmp1_b32 s68, 6\n"
  "s_cbranch_scc0 Lscan%=\n"
  "s_bitset0_b32 s68, 6\n"
  "buffer_load_dwordx2 v[76:77], v9, s[56:59], s26 offen sc1\n"
  "buffer_load_dwordx4 v[176:179], v8, s[56:59], s26 offen offset:0 sc1\n"
  "buffer_load_dwordx4 v[180:183], v8, s[56:59], s26 offen offset:1024 sc1\n"
  "Lls7%=:\n"
  "s_bitcmp1_b32 s68, 7\n"
  "s_cbranch_scc0 Lscan%=\n"
  "s_bitset0_b32 s68, 7\n"
  "buffer_load_dwordx2 v[78:79], v9, s[56:59], s27 offen sc1\n"
  "buffer_load_dwordx4 v[184:187], v8, s[56:59], s27 offen offset:0 sc1\n"
  "buffer_load_dwordx4 v[188:191], v8, s[56:59], s27 offen offset:1024 sc1\n"
  "Lls8%=:\n"
  "s_bitcmp1_b32 s68, 8\n"
  "s_cbranch_scc0 Lscan%=\n"
  "s_bitset0_b32 s68, 8\n"
  "buffer_load_dwordx2 v[80:81], v9, s[56:59], s28 offen sc1\n"
  "buffer_load_dwordx4 v[192:195], v8, s[56:59], s28 offen offset:0 sc1\n"
  "buffer_load_dwordx4 v[196:199], v8, s[56:59], s28 offen offset:1024 sc1\n"
  "Lls9%=:\n"
  "s_bitcmp1_b32 s68, 9\n"
  "s_cbranch_scc0 Lscan%=\n"
  "s_bitset0_b32 s68, 9\n"
  "buffer_load_dwordx2 v[82:83], v9, s[56:59], s29 offen sc1\n"
  "buffer_load_dwordx4 v[200:203], v8, s[56:59], s29 offen offset:0 sc1\n"
  "buffer_load_dwordx4 v[204:207], v8, s[56:59], s29 offen offset:1024 sc1\n"
  "Lls10%=:\n"
  "s_bitcmp1_b32 s68, 10\n"
  "s_cbranch_scc0 Lscan%=\n"
  "s_bitset0_b32 s68, 10\n"
  "buffer_load_dwordx2 v[84:85], v9, s[56:59], s30 offen sc1\n"
  "buffer_load_dwordx4 v[208:211], v8, s[56:59], s30 offen offset:0 sc1\n"
  "buffer_load_dwordx4 v[212:215], v8, s[56:59], s30 offen offset:1024 sc1\n"
  "Lls11%=:\n"
  "s_bitcmp1_b32 s68, 11\n"
  "s_cbranch_scc0 Lscan%=\n"
  "s_bitset0_b32 s68, 11\n"
  "buffer_load_dwordx2 v[86:87], v9, s[56:59], s31 offen sc1\n"
  "buffer_load_dwordx4 v[216:219], v8, s[56:59], s31 offen offset:0 sc1\n"
  "buffer_load_dwordx4 v[220:223], v8, s[56:59], s31 offen offset:1024 sc1\n"
  "Lnext%=:\n"
  "s_cmp_lg_u64 s[36:37], 0\n"
  "s_cbranch_scc1 Lpoll%=\n"
  "Lgive%=:\n"
  "s_cmp_lg_u32 s66, 252\n"
  "s_cbranch_scc1 Lw0%=\n"
  "buffer_load_dwordx2 v[12:13], v11, s[56:59], s88 offen sc1\n"
  "s_waitcnt vmcnt(0)\n"
  "Lw0%=:\n"
  "s_mov_b32 s12, 2\n"
  "s_cmp_gt_u32 s17, 12\n"
  "s_waitcnt vmcnt(2)\n"
  "s_cbranch_scc1 Lupd%=\n"
  "v_max3_f32 v48, v64, v66, v68\n"
  "v_max3_f32 v49, v70, v72, v74\n"
  "v_max3_f32 v50, v76, v78, v80\n"
  "v_max3_f32 v51, v82, v84, v86\n"
  "v_max3_f32 v48, v48, v49, v50\n"
  "v_max_f32 v48, v48, v51\n"
  "v_sub_f32 v64, v64, v48\n"
  "v_sub_f32 v66, v66, v48\n"
  "v_sub_f32 v68, v68, v48\n"
  "v_sub_f32 v70, v70, v48\n"
  "v_sub_f32 v72, v72, v48\n"
  "v_sub_f32 v74, v74, v48\n"
  "v_sub_f32 v76, v76, v48\n"
  "v_sub_f32 v78, v78, v48\n"
  "v_sub_f32 v80, v80, v48\n"
  "v_sub_f32 v82, v82, v48\n"
  "v_sub_f32 v84, v84, v48\n"
  "v_sub_f32 v86, v86, v48\n"
  "v_exp_f32 v64, v64\n"
  "v_exp_f32 v66, v66\n"
  "v_exp_f32 v68, v68\n"
  "v_exp_f32 v70, v70\n"
  "v_exp_f32 v72, v72\n"
  "v_exp_f32 v74, v74\n"
  "v_exp_f32 v76, v76\n"
  "v_exp_f32 v78, v78\n"
  "v_exp_f32 v80, v80\n"
  "v_exp_f32 v82, v82\n"
  "v_exp_f32 v84, v84\n"
  "v_exp_f32 v86, v86\n"
  "v_mul_f32 v14, v64, v65\n"
  "v_mul_f32 v15, v66, v67\n"
  "v_fmac_f32 v14, v68, v69\n"
  "v_fmac_f32 v15, v70, v71\n"
  "v_fmac_f32 v14, v72, v73\n"
  "v_fmac_f32 v15, v74, v75\n"
  "v_fmac_f32 v14, v76, v77\n"
  "v_fmac_f32 v15, v78, v79\n"
  "v_fmac_f32 v14, v80, v81\n"
  "v_fmac_f32 v15, v82, v83\n"
  "v_fmac_f32 v14, v84, v85\n"
  "v_fmac_f32 v15, v86, v87\n"
  "v_add_f32 v14, v14, v15\n"
  "v_rcp_f32 v14, v14\n"
  "s_waitcnt vmcnt(0)\n"
  "s_setpc_b64 s[80:81]\n"
  "Lcs11%=:\n"
  "v_lshlrev_b32 v48, 16, v216\n"
  "v_and_b32 v49, s82, v216\n"
  "v_lshlrev_b32 v50, 16, v217\n"
  "v_and_b32 v51, s82, v217\n"
  "v_lshlrev_b32 v52, 16, v218\n"
  "v_and_b32 v53, s82, v218\n"
  "v_lshlrev_b32 v54, 16, v219\n"
  "v_and_b32 v55, s82, v219\n"
  "v_lshlrev_b32 v56, 16, v220\n"
  "v_and_b32 v57, s82, v220\n"
  "v_lshlrev_b32 v58, 16, v221\n"
  "v_and_b32 v59, s82, v221\n"
  "v_lshlrev_b32 v60, 16, v222\n"
  "v_and_b32 v61, s82, v222\n"
  "v_lshlrev_b32 v62, 16, v223\n"
  "v_and_b32 v63, s82, v223\n"
  "v_pk_fma_f32 v[32:33], v[48:49], v[86:87], v[32:33] op_sel_hi:[1,0,1]\n"
  "v_pk_fma_f32 v[34:35], v[50:51], v[86:87], v[34:35] op_sel_hi:[1,0,1]\n"
  "v_pk_fma_f32 v[36:37], v[52:53], v[86:87], v[36:37] op_sel_hi:[1,0,1]\n"
  "v_pk_fma_f32 v[38:39], v[54:55], v[86:87], v[38:39] op_sel_hi:[1,0,1]\n"
  "v_pk_fma_f32 v[40:41], v[56:57], v[86:87], v[40:41] op_sel_hi:[1,0,1]\n"
  "v_pk_fma_f32 v[42:43], v[58:59], v[86:87], v[42:43] op_sel_hi:[1,0,1]\n"
  "v_pk_fma_f32 v[44:45], v[60:61], v[86:87], v[44:45] op_sel_hi:[1,0,1]\n"
  "v_pk_fma_f32 v[46:47], v[62:63], v[86:87], v[46:47] op_sel_hi:[1,0,1]\n"
  "Lcs10%=:\n"
  "v_lshlrev_b32 v48, 16, v208\n"
  "v_and_b32 v49, s82, v208\n"
  "v_lshlrev_b32 v50, 16, v209\n"
  "v_and_b32 v51, s82, v209\n"
  "v_lshlrev_b32 v52, 16, v210\n"
  "v_and_b32 v53, s82, v210\n"
  "v_lshlrev_b32 v54, 16, v211\n"
  "v_and_b32 v55, s82, v211\n"
  "v_lshlrev_b32 v56, 16, v212\n"
  "v_and_b32 v57, s82, v212\n"
  "v_lshlrev_b32 v58, 16, v213\n"
  "v_and_b32 v59, s82, v213\n"
  "v_lshlrev_b32 v60, 16, v214\n"
  "v_and_b32 v61, s82, v214\n"
  "v_lshlrev_b32 v62, 16, v215\n"
  "v_and_b32 v63, s82, v215\n"
  "v_pk_fma_f32 v[32:33], v[48:49], v[84:85], v[32:33] op_sel_hi:[1,0,1]\n"
  "v_pk_fma_f32 v[34:35], v[50:51], v[84:85], v[34:35] op_sel_hi:[1,0,1]\n"
  "v_pk_fma_f32 v[36:37], v[52:53], v[84:85], v[36:37] op_sel_hi:[1,0,1]\n"
  "v_pk_fma_f32 v[38:39], v[54:55], v[84:85], v[38:39] op_sel_hi:[1,0,1]\n"
  "v_pk_fma_f32 v[40:41], v[56:57], v[84:85], v[40:41] op_sel_hi:[1,0,1]\n"
  "v_pk_fma_f32 v[42:43], v[58:59], v[84:85], v[42:43] op_sel_hi:[1,0,1]\n"
  "v_pk_fma_f32 v[44:45], v[60:61], v[84:85], v[44:45] op_sel_hi:[1,0,1]\n"
  "v_pk_fma_f32 v[46:47], v[62:63], v[84:85], v[46:47] op_sel_hi:[1,0,1]\n"
  "Lcs9%=:\n"
  "v_lshlrev_b32 v48, 16, v200\n"
  "v_and_b32 v49, s82, v200\n"
  "v_lshlrev_b32 v50, 16, v201\n"
  "v_and_b32 v51, s82, v201\n"
  "v_lshlrev_b32 v52, 16, v202\n"
  "v_and_b32 v53, s82, v202\n"
  "v_lshlrev_b32 v54, 16, v203\n"
  "v_and_b32 v55, s82, v203\n"
  "v_lshlrev_b32 v56, 16, v204\n"
  "v_and_b32 v57, s82, v204\n"
  "v_lshlrev_b32 v58, 16, v205\n"
  "v_and_b32 v59, s82, v205\n"
  "v_lshlrev_b32 v60, 16, v206\n"
  "v_and_b32 v61, s82, v206\n"
  "v_lshlrev_b32 v62, 16, v207\n"
  "v_and_b32 v63, s82, v207\n"
  "v_pk_fma_f32 v[32:33], v[48:49], v[82:83], v[32:33] op_sel_hi:[1,0,1]\n"
  "v_pk_fma_f32 v[34:35], v[50:51], v[82:83], v[34:35] op_sel_hi:[1,0,1]\n"
  "v_pk_fma_f32 v[36:37], v[52:53], v[82:83], v[36:37] op_sel_hi:[1,0,1]\n"
  "v_pk_fma_f32 v[38:39], v[54:55], v[82:83], v[38:39] op_sel_hi:[1,0,1]\n"
  "v_pk_fma_f32 v[40:41], v[56:57], v[82:83], v[40:41] op_sel_hi:[1,0,1]\n"
  "v_pk_fma_f32 v[42:43], v[58:59], v[82:83], v[42:43] op_sel_hi:[1,0,1]\n"
  "v_pk_fma_f32 v[44:45], v[60:61], v[82:83], v[44:45] op_sel_hi:[1,0,1]\n"
  "v_pk_fma_f32 v[46:47], v[62:63], v[82:83], v[46:47] op_sel_hi:[1,0,1]\n"
  "Lcs8%=:\n"
  "v_lshlrev_b32 v48, 16, v192\n"
  "v_and_b32 v49, s82, v192\n"
  "v_lshlrev_b32 v50, 16, v193\n"
  "v_and_b32 v51, s82, v193\n"
  "v_lshlrev_b32 v52, 16, v194\n"
  "v_and_b32 v53, s82, v194\n"
  "v_lshlrev_b32 v54, 16, v195\n"
  "v_and_b32 v55, s82, v195\n"
  "v_lshlrev_b32 v56, 16, v196\n"
  "v_and_b32 v57, s82, v196\n"
  "v_lshlrev_b32 v58, 16, v197\n"
  "v_and_b32 v59, s82, v197\n"
  "v_lshlrev_b32 v60, 16, v198\n"
  "v_and_b32 v61, s82, v198\n"
  "v_lshlrev_b32 v62, 16, v199\n"
  "v_and_b32 v63, s82, v199\n"
  "v_pk_fma_f32 v[32:33], v[48:49], v[80:81], v[32:33] op_sel_hi:[1,0,1]\n"
  "v_pk_fma_f32 v[34:35], v[50:51], v[80:81], v[34:35] op_sel_hi:[1,0,1]\n"
  "v_pk_fma_f32 v[36:37], v[52:53], v[80:81], v[36:37] op_sel_hi:[1,0,1]\n"
  "v_pk_fma_f32 v[38:39], v[54:55], v[80:81], v[38:39] op_sel_hi:[1,0,1]\n"
  "v_pk_fma_f32 v[40:41], v[56:57], v[80:81], v[40:41] op_sel_hi:[1,0,1]\n"
  "v_pk_fma_f32 v[42:43], v[58:59], v[80:81], v[42:43] op_sel_hi:[1,0,1]\n"
  "v_pk_fma_f32 v[44:45], v[60:61], v[80:81], v[44:45] op_sel_hi:[1,0,1]\n"
  "v_pk_fma_f32 v[46:47], v[62:63], v[80:81], v[46:47] op_sel_hi:[1,0,1]\n"
  "Lcs7%=:\n"
  "v_lshlrev_b32 v48, 16, v184\n"
  "v_and_b32 v49, s82, v184\n"
  "v_lshlrev_b32 v50, 16, v185\n"
  "v_and_b32 v51, s82, v185\n"
  "v_lshlrev_b32 v52, 16, v186\n"
  "v_and_b32 v53, s82, v186\n"
  "v_lshlrev_b32 v54, 16, v187\n"
  "v_and_b32 v55, s82, v187\n"
  "v_lshlrev_b32 v56, 16, v188\n"
  "v_and_b32 v57, s82, v188\n"
  "v_lshlrev_b32 v58, 16, v189\n"
  "v_and_b32 v59, s82, v189\n"
  "v_lshlrev_b32 v60, 16, v190\n"
  "v_and_b32 v61, s82, v190\n"
  "v_lshlrev_b32 v62, 16, v191\n"
  "v_and_b32 v63, s82, v191\n"
  "v_pk_fma_f32 v[32:33], v[48:49], v[78:79], v[32:33] op_sel_hi:[1,0,1]\n"
  "v_pk_fma_f32 v[34:35], v[50:51], v[78:79], v[34:35] op_sel_hi:[1,0,1]\n"
  "v_pk_fma_f32 v[36:37], v[52:53], v[78:79], v[36:37] op_sel_hi:[1,0,1]\n"
  "v_pk_fma_f32 v[38:39], v[54:55], v[78:79], v[38:39] op_sel_hi:[1,0,1]\n"
  "v_pk_fma_f32 v[40:41], v[56:57], v[78:79], v[40:41] op_sel_hi:[1,0,1]\n"
  "v_pk_fma_f32 v[42:43], v[58:59], v[78:79], v[42:43] op_sel_hi:[1,0,1]\n"
  "v_pk_fma_f32 v[44:45], v[60:61], v[78:79], v[44:45] op_sel_hi:[1,0,1]\n"
  "v_pk_fma_f32 v[46:47], v[62:63], v[78:79], v[46:47] op_sel_hi:[1,0,1]\n"
  "Lcs6%=:\n"
  "v_lshlrev_b32 v48, 16, v176\n"
  "v_and_b32 v49, s82, v176\n"
  "v_lshlrev_b32 v50, 16, v177\n"
  "v_and_b32 v51, s82, v177\n"
  "v_lshlrev_b32 v52, 16, v178\n"
  "v_and_b32 v53, s82, v178\n"
  "v_lshlrev_b32 v54, 16, v179\n"
  "v_and_b32 v55, s82, v179\n"
  "v_lshlrev_b32 v56, 16, v180\n"
  "v_and_b32 v57, s82, v180\n"
  "v_lshlrev_b32 v58, 16, v181\n"
  "v_and_b32 v59, s82, v181\n"
  "v_lshlrev_b32 v60, 16, v182\n"
  "v_and_b32 v61, s82, v182\n"
  "v_lshlrev_b32 v62, 16, v183\n"
  "v_and_b32 v63, s82, v183\n"
  "v_pk_fma_f32 v[32:33], v[48:49], v[76:77], v[32:33] op_sel_hi:[1,0,1]\n"
  "v_pk_fma_f32 v[34:35], v[50:51], v[76:77], v[34:35] op_sel_hi:[1,0,1]\n"
  "v_pk_fma_f32 v[36:37], v[52:53], v[76:77], v[36:37] op_sel_hi:[1,0,1]\n"
  "v_pk_fma_f32 v[38:39], v[54:55], v[76:77], v[38:39] op_sel_hi:[1,0,1]\n"
  "v_pk_fma_f32 v[40:41], v[56:57], v[76:77], v[40:41] op_sel_hi:[1,0,1]\n"
  "v_pk_fma_f32 v[42:43], v[58:59], v[76:77], v[42:43] op_sel_hi:[1,0,1]\n"
  "v_pk_fma_f32 v[44:45], v[60:61], v[76:77], v[44:45] op_sel_hi:[1,0,1]\n"
  "v_pk_fma_f32 v[46:47], v[62:63], v[76:77], v[46:47] op_sel_hi:[1,0,1]\n"
  "Lcs5%=:\n"
  "v_lshlrev_b32 v48, 16, v168\n"
  "v_and_b32 v49, s82, v168\n"
  "v_lshlrev_b32 v50, 16, v169\n"
  "v_and_b32 v51, s82, v169\n"
  "v_lshlrev_b32 v52, 16, v170\n"
  "v_and_b32 v53, s82, v170\n"
  "v_lshlrev_b32 v54, 16, v171\n"
  "v_and_b32 v55, s82, v171\n"
  "v_lshlrev_b32 v56, 16, v172\n"
  "v_and_b32 v57, s82, v172\n"
  "v_lshlrev_b32 v58, 16, v173\n"
  "v_and_b32 v59, s82, v173\n"
  "v_lshlrev_b32 v60, 16, v174\n"
  "v_and_b32 v61, s82, v174\n"
  "v_lshlrev_b32 v62, 16, v175\n"
  "v_and_b32 v63, s82, v175\n"
  "v_pk_fma_f32 v[32:33], v[48:49], v[74:75], v[32:33] op_sel_hi:[1,0,1]\n"
  "v_pk_fma_f32 v[34:35], v[50:51], v[74:75], v[34:35] op_sel_hi:[1,0,1]\n"
  "v_pk_fma_f32 v[36:37], v[52:53], v[74:75], v[36:37] op_sel_hi:[1,0,1]\n"
  "v_pk_fma_f32 v[38:39], v[54:55], v[74:75], v[38:39] op_sel_hi:[1,0,1]\n"
  "v_pk_fma_f32 v[40:41], v[56:57], v[74:75], v[40:41] op_sel_hi:[1,0,1]\n"
  "v_pk_fma_f32 v[42:43], v[58:59], v[74:75], v[42:43] op_sel_hi:[1,0,1]\n"
  "v_pk_fma_f32 v[44:45], v[60:61], v[74:75], v[44:45] op_sel_hi:[1,0,1]\n"
  "v_pk_fma_f32 v[46:47], v[62:63], v[74:75], v[46:47] op_sel_hi:[1,0,1]\n"
  "Lcs4%=:\n"
  "v_lshlrev_b32 v48, 16, v160\n"
  "v_and_b32 v49, s82, v160\n"
  "v_lshlrev_b32 v50, 16, v161\n"
  "v_and_b32 v51, s82, v161\n"
  "v_lshlrev_b32 v52, 16, v162\n"
  "v_and_b32 v53, s82, v162\n"
  "v_lshlrev_b32 v54, 16, v163\n"
  "v_and_b32 v55, s82, v163\n"
  "v_lshlrev_b32 v56, 16, v164\n"
  "v_and_b32 v57, s82, v164\n"
  "v_lshlrev_b32 v58, 16, v165\n"
  "v_and_b32 v59, s82, v165\n"
  "v_lshlrev_b32 v60, 16, v166\n"
  "v_and_b32 v61, s82, v166\n"
  "v_lshlrev_b32 v62, 16, v167\n"
  "v_and_b32 v63, s82, v167\n"
  "v_pk_fma_f32 v[32:33], v[48:49], v[72:73], v[32:33] op_sel_hi:[1,0,1]\n"
  "v_pk_fma_f32 v[34:35], v[50:51], v[72:73], v[34:35] op_sel_hi:[1,0,1]\n"
  "v_pk_fma_f32 v[36:37], v[52:53], v[72:73], v[36:37] op_sel_hi:[1,0,1]\n"
  "v_pk_fma_f32 v[38:39], v[54:55], v[72:73], v[38:39] op_sel_hi:[1,0,1]\n"
  "v_pk_fma_f32 v[40:41], v[56:57], v[72:73], v[40:41] op_sel_hi:[1,0,1]\n"
  "v_pk_fma_f32 v[42:43], v[58:59], v[72:73], v[42:43] op_sel_hi:[1,0,1]\n"
  "v_pk_fma_f32 v[44:45], v[60:61], v[72:73], v[44:45] op_sel_hi:[1,0,1]\n"
  "v_pk_fma_f32 v[46:47], v[62:63], v[72:73], v[46:47] op_sel_hi:[1,0,1]\n"
  "Lcs3%=:\n"
  "v_lshlrev_b32 v48, 16, v152\n"
  "v_and_b32 v49, s82, v152\n"
  "v_lshlrev_b32 v50, 16, v153\n"
  "v_and_b32 v51, s82, v153\n"
  "v_lshlrev_b32 v52, 16, v154\n"
  "v_and_b32 v53, s82, v154\n"
  "v_lshlrev_b32 v54, 16, v155\n"
  "v_and_b32 v55, s82, v155\n"
  "v_lshlrev_b32 v56, 16, v156\n"
  "v_and_b32 v57, s82, v156\n"
  "v_lshlrev_b32 v58, 16, v157\n"
  "v_and_b32 v59, s82, v157\n"
  "v_lshlrev_b32 v60, 16, v158\n"
  "v_and_b32 v61, s82, v158\n"
  "v_lshlrev_b32 v62, 16, v159\n"
  "v_and_b32 v63, s82, v159\n"
  "v_pk_fma_f32 v[32:33], v[48:49], v[70:71], v[32:33] op_sel_hi:[1,0,1]\n"
  "v_pk_fma_f32 v[34:35], v[50:51], v[70:71], v[34:35] op_sel_hi:[1,0,1]\n"
  "v_pk_fma_f32 v[36:37], v[52:53], v[70:71], v[36:37] op_sel_hi:[1,0,1]\n"
  "v_pk_fma_f32 v[38:39], v[54:55], v[70:71], v[38:39] op_sel_hi:[1,0,1]\n"
  "v_pk_fma_f32 v[40:41], v[56:57], v[70:71], v[40:41] op_sel_hi:[1,0,1]\n"
  "v_pk_fma_f32 v[42:43], v[58:59], v[70:71], v[42:43] op_sel_hi:[1,0,1]\n"
  "v_pk_fma_f32 v[44:45], v[60:61], v[70:71], v[44:45] op_sel_hi:[1,0,1]\n"
  "v_pk_fma_f32 v[46:47], v[62:63], v[70:71], v[46:47] op_sel_hi:[1,0,1]\n"
  "Lcs2%=:\n"
  "v_lshlrev_b32 v48, 16, v144\n"
  "v_and_b32 v49, s82, v144\n"
  "v_lshlrev_b32 v50, 16, v145\n"
  "v_and_b32 v51, s82, v145\n"
  "v_lshlrev_b32 v52, 16, v146\n"
  "v_and_b32 v53, s82, v146\n"
  "v_lshlrev_b32 v54, 16, v147\n"
  "v_and_b32 v55, s82, v147\n"
  "v_lshlrev_b32 v56, 16, v148\n"
  "v_and_b32 v57, s82, v148\n"
  "v_lshlrev_b32 v58, 16, v149\n"
  "v_and_b32 v59, s82, v149\n"
  "v_lshlrev_b32 v60, 16, v150\n"
  "v_and_b32 v61, s82, v150\n"
  "v_lshlrev_b32 v62, 16, v151\n"
  "v_and_b32 v63, s82, v151\n"
  "v_pk_fma_f32 v[32:33], v[48:49], v[68:69], v[32:33] op_sel_hi:[1,0,1]\n"
  "v_pk_fma_f32 v[34:35], v[50:51], v[68:69], v[34:35] op_sel_hi:[1,0,1]\n"
  "v_pk_fma_f32 v[36:37], v[52:53], v[68:69], v[36:37] op_sel_hi:[1,0,1]\n"
  "v_pk_fma_f32 v[38:39], v[54:55], v[68:69], v[38:39] op_sel_hi:[1,0,1]\n"
  "v_pk_fma_f32 v[40:41], v[56:57], v[68:69], v[40:41] op_sel_hi:[1,0,1]\n"
  "v_pk_fma_f32 v[42:43], v[58:59], v[68:69], v[42:43] op_sel_hi:[1,0,1]\n"
  "v_pk_fma_f32 v[44:45], v[60:61], v[68:69], v[44:45] op_sel_hi:[1,0,1]\n"
  "v_pk_fma_f32 v[46:47], v[62:63], v[68:69], v[46:47] op_sel_hi:[1,0,1]\n"
  "Lcs1%=:\n"
  "v_lshlrev_b32 v48, 16, v136\n"
  "v_and_b32 v49, s82, v136\n"
  "v_lshlrev_b32 v50, 16, v137\n"
  "v_and_b32 v51, s82, v137\n"
  "v_lshlrev_b32 v52, 16, v138\n"
  "v_and_b32 v53, s82, v138\n"
  "v_lshlrev_b32 v54, 16, v139\n"
  "v_and_b32 v55, s82, v139\n"
  "v_lshlrev_b32 v56, 16, v140\n"
  "v_and_b32 v57, s82, v140\n"
  "v_lshlrev_b32 v58, 16, v141\n"
  "v_and_b32 v59, s82, v141\n"
  "v_lshlrev_b32 v60, 16, v142\n"
  "v_and_b32 v61, s82, v142\n"
  "v_lshlrev_b32 v62, 16, v143\n"
  "v_and_b32 v63, s82, v143\n"
  "v_pk_fma_f32 v[32:33], v[48:49], v[66:67], v[32:33] op_sel_hi:[1,0,1]\n"
  "v_pk_fma_f32 v[34:35], v[50:51], v[66:67], v[34:35] op_sel_hi:[1,0,1]\n"
  "v_pk_fma_f32 v[36:37], v[52:53], v[66:67], v[36:37] op_sel_hi:[1,0,1]\n"
  "v_pk_fma_f32 v[38:39], v[54:55], v[66:67], v[38:39] op_sel_hi:[1,0,1]\n"
  "v_pk_fma_f32 v[40:41], v[56:57], v[66:67], v[40:41] op_sel_hi:[1,0,1]\n"
  "v_pk_fma_f32 v[42:43], v[58:59], v[66:67], v[42:43] op_sel_hi:[1,0,1]\n"
  "v_pk_fma_f32 v[44:45], v[60:61], v[66:67], v[44:45] op_sel_hi:[1,0,1]\n"
  "v_pk_fma_f32 v[46:47], v[62:63], v[66:67], v[46:47] op_sel_hi:[1,0,1]\n"
  "Lcs0%=:\n"
  "v_lshlrev_b32 v48, 16, v128\n"
  "v_and_b32 v49, s82, v128\n"
  "v_lshlrev_b32 v50, 16, v129\n"
  "v_and_b32 v51, s82, v129\n"
  "v_lshlrev_b32 v52, 16, v130\n"
  "v_and_b32 v53, s82, v130\n"
  "v_lshlrev_b32 v54, 16, v131\n"
  "v_and_b32 v55, s82, v131\n"
  "v_lshlrev_b32 v56, 16, v132\n"
  "v_and_b32 v57, s82, v132\n"
  "v_lshlrev_b32 v58, 16, v133\n"
  "v_and_b32 v59, s82, v133\n"
  "v_lshlrev_b32 v60, 16, v134\n"
  "v_and_b32 v61, s82, v134\n"
  "v_lshlrev_b32 v62, 16, v135\n"
  "v_and_b32 v63, s82, v135\n"
  "v_pk_fma_f32 v[32:33], v[48:49], v[64:65], v[32:33] op_sel_hi:[1,0,1]\n"
  "v_pk_fma_f32 v[34:35], v[50:51], v[64:65], v[34:35] op_sel_hi:[1,0,1]\n"
  "v_pk_fma_f32 v[36:37], v[52:53], v[64:65], v[36:37] op_sel_hi:[1,0,1]\n"
  "v_pk_fma_f32 v[38:39], v[54:55], v[64:65], v[38:39] op_sel_hi:[1,0,1]\n"
  "v_pk_fma_f32 v[40:41], v[56:57], v[64:65], v[40:41] op_sel_hi:[1,0,1]\n"
  "v_pk_fma_f32 v[42:43], v[58:59], v[64:65], v[42:43] op_sel_hi:[1,0,1]\n"
  "v_pk_fma_f32 v[44:45], v[60:61], v[64:65], v[44:45] op_sel_hi:[1,0,1]\n"
  "v_pk_fma_f32 v[46:47], v[62:63], v[64:65], v[46:47] op_sel_hi:[1,0,1]\n"
  "Lfin%=:\n"
  "v_pk_mul_f32 v[32:33], v[32:33], v[14:15] op_sel_hi:[1,0]\n"
  "v_pk_mul_f32 v[34:35], v[34:35], v[14:15] op_sel_hi:[1,0]\n"
  "v_pk_mul_f32 v[36:37], v[36:37], v[14:15] op_sel_hi:[1,0]\n"
  "v_pk_mul_f32 v[38:39], v[38:39], v[14:15] op_sel_hi:[1,0]\n"
  "v_pk_mul_f32 v[40:41], v[40:41], v[14:15] op_sel_hi:[1,0]\n"
  "v_pk_mul_f32 v[42:43], v[42:43], v[14:15] op_sel_hi:[1,0]\n"
  "v_pk_mul_f32 v[44:45], v[44:45], v[14:15] op_sel_hi:[1,0]\n"
  "v_pk_mul_f32 v[46:47], v[46:47], v[14:15] op_sel_hi:[1,0]\n"
  "v_cvt_pk_bf16_f32 v48, v32, v33\n"
  "v_cvt_pk_bf16_f32 v49, v34, v35\n"
  "v_cvt_pk_bf16_f32 v50, v36, v37\n"
  "v_cvt_pk_bf16_f32 v51, v38, v39\n"
  "v_cvt_pk_bf16_f32 v52, v40, v41\n"
  "v_cvt_pk_bf16_f32 v53, v42, v43\n"
  "v_cvt_pk_bf16_f32 v54, v44, v45\n"
  "v_cvt_pk_bf16_f32 v55, v46, v47\n"
  "v_permlane32_swap_b32 v48, v50\n"
  "v_permlane32_swap_b32 v49, v51\n"
  "v_permlane32_swap_b32 v52, v54\n"
  "v_permlane32_swap_b32 v53, v55\n"
  "v_permlane16_swap_b32 v48, v50\n"
  "v_permlane16_swap_b32 v49, v51\n"
  "v_permlane16_swap_b32 v52, v54\n"
  "v_permlane16_swap_b32 v53, v55\n"
  "s_mov_b32 s12, 0\n"
  "global_store_dwordx4 v10, v[48:51], s[62:63] offset:0\n"
  "global_store_dwordx4 v10, v[52:55], s[62:63] offset:64\n"
  "Lupd%=:\n"
  "s_cmp_lg_u32 s66, 252\n"
  "s_cbranch_scc1 Lend%=\n"
  "Lu%=:\n"
  "v_cmp_eq_u64 vcc, s[38:39], v[12:13]\n"
  "s_nop 0\n"
  "s_cmp_eq_u64 vcc, -1\n"
  "s_cbranch_scc1 Lg%=\n"
  "buffer_load_dwordx2 v[12:13], v11, s[56:59], s88 offen sc1\n"
  "s_sub_u32 s89, s89, 1\n"
  "s_cmp_gt_i32 s89, 0\n"
  "s_waitcnt vmcnt(0)\n"
  "s_cbranch_scc1 Lu%=\n"
  "Lg%=:\n"
  "s_mov_b64 exec, 1\n"
  "buffer_store_dwordx2 v[26:27], off, s[56:59], s67 sc1\n"
  "s_mov_b64 exec, -1\n"
  "s_branch Lend%=\n"
  "Lhre%=:\n"
  "v_lshlrev_b32 v8, 3, v24\n"
  "buffer_load_dwordx2 v[8:9], v8, s[56:59], s67 offen offset:512 sc1\n"
  "s_sub_u32 s89, s89, 1\n"
  "s_waitcnt vmcnt(0)\n"
  "s_cbranch_scc0 Lhint%=\n"
  "s_branch Lhok%=\n"
  "Lsp0%=:\n"
  "s_call_b64 s[70:71], Lmask0%=\n"
  "s_branch Lbp0%=\n"
  "Lsp2%=:\n"
  "s_call_b64 s[70:71], Lmask1%=\n"
  "s_branch Lbp2%=\n"
  "Lsp1%=:\n"
  "s_call_b64 s[70:71], Lmask0%=\n"
  "s_branch Lbp1%=\n"
  "Lsm1%=:\n"
  "s_call_b64 s[70:71], Lmask0%=\n"
  "s_branch Lbm1%=\n"
  "Lsm0%=:\n"
  "s_call_b64 s[70:71], Lmask0%=\n"
  "s_branch Lbm0%=\n"
  "Lsf%=:\n"
  "s_call_b64 s[70:71], Lmask1%=\n"
  "s_branch Lbf%=\n"
  "Lmask0%=:\n"
  "s_nop 7\n"
  "v_mov_b32 v30, 0xff800000\n"
  "v_cmp_lt_i32 s[72:73], 0, v21\n"
  "s_nop 1\n"
  "v_cmp_lt_i32 s[86:87], 4, v21\n"
  "v_cndmask_b32 v64, v30, v64, s[72:73]\n"
  "v_cndmask_b32 v72, v30, v72, s[72:73]\n"
  "v_cmp_lt_i32 s[88:89], 8, v21\n"
  "v_cndmask_b32 v65, v30, v65, s[86:87]\n"
  "v_cndmask_b32 v73, v30, v73, s[86:87]\n"
  "v_cmp_lt_i32 s[72:73], 12, v21\n"
  "v_cndmask_b32 v66, v30, v66, s[88:89]\n"
  "v_cndmask_b32 v74, v30, v74, s[88:89]\n"
  "v_cmp_lt_i32 s[86:87], 16, v21\n"
  "v_cndmask_b32 v67, v30, v67, s[72:73]\n"
  "v_cndmask_b32 v75, v30, v75, s[72:73]\n"
  "v_cmp_lt_i32 s[88:89], 20, v21\n"
  "v_cndmask_b32 v68, v30, v68, s[86:87]\n"
  "v_cndmask_b32 v76, v30, v76, s[86:87]\n"
  "v_cmp_lt_i32 s[72:73], 24, v21\n"
  "v_cndmask_b32 v69, v30, v69, s[88:89]\n"
  "v_cndmask_b32 v77, v30, v77, s[88:89]\n"
  "v_cmp_lt_i32 s[86:87], 28, v21\n"
  "v_cndmask_b32 v70, v30, v70, s[72:73]\n"
  "v_cndmask_b32 v78, v30, v78, s[72:73]\n"
  "v_cmp_lt_i32 s[88:89], 32, v21\n"
  "v_cndmask_b32 v71, v30, v71, s[86:87]\n"
  "v_cndmask_b32 v79, v30, v79, s[86:87]\n"
  "v_cmp_lt_i32 s[72:73], 36, v21\n"
  "v_cndmask_b32 v80, v30, v80, s[88:89]\n"
  "v_cndmask_b32 v88, v30, v88, s[88:89]\n"
  "v_cmp_lt_i32 s[86:87], 40, v21\n"
  "v_cndmask_b32 v81, v30, v81, s[72:73]\n"
  "v_cndmask_b32 v89, v30, v89, s[72:73]\n"
  "v_cmp_lt_i32 s[88:89], 44, v21\n"
  "v_cndmask_b32 v82, v30, v82, s[86:87]\n"
  "v_cndmask_b32 v90, v30, v90, s[86:87]\n"
  "v_cmp_lt_i32 s[72:73], 48, v21\n"
  "v_cndmask_b32 v83, v30, v83, s[88:89]\n"
  "v_cndmask_b32 v91, v30, v91, s[88:89]\n"
  "v_cmp_lt_i32 s[86:87], 52, v21\n"
  "v_cndmask_b32 v84, v30, v84, s[72:73]\n"
  "v_cndmask_b32 v92, v30, v92, s[72:73]\n"
  "v_cmp_lt_i32 s[88:89], 56, v21\n"
  "v_cndmask_b32 v85, v30, v85, s[86:87]\n"
  "v_cndmask_b32 v93, v30, v93, s[86:87]\n"
  "v_cmp_lt_i32 s[72:73], 60, v21\n"
  "v_cndmask_b32 v86, v30, v86, s[88:89]\n"
  "v_cndmask_b32 v94, v30, v94, s[88:89]\n"
  "v_cndmask_b32 v87, v30, v87, s[72:73]\n"
  "v_cndmask_b32 v95, v30, v95, s[72:73]\n"
  "s_nop 1\n"
  "s_setpc_b64 s[70:71]\n"
  "Lmask1%=:\n"
  "s_nop 7\n"
  "v_mov_b32 v30, 0xff800000\n"
  "v_cmp_lt_i32 s[72:73], 0, v21\n"
  "s_nop 1\n"
  "v_cmp_lt_i32 s[86:87], 4, v21\n"
  "v_cndmask_b32 v96, v30, v96, s[72:73]\n"
  "v_cndmask_b32 v104, v30, v104, s[72:73]\n"
  "v_cmp_lt_i32 s[88:89], 8, v21\n"
  "v_cndmask_b32 v97, v30, v97, s[86:87]\n"
  "v_cndmask_b32 v105, v30, v105, s[86:87]\n"
  "v_cmp_lt_i32 s[72:73], 12, v21\n"
  "v_cndmask_b32 v98, v30, v98, s[88:89]\n"
  "v_cndmask_b32 v106, v30, v106, s[88:89]\n"
  "v_cmp_lt_i32 s[86:87], 16, v21\n"
  "v_cndmask_b32 v99, v30, v99, s[72:73]\n"
  "v_cndmask_b32 v107, v30, v107, s[72:73]\n"
  "v_cmp_lt_i32 s[88:89], 20, v21\n"
  "v_cndmask_b32 v100, v30, v100, s[86:87]\n"
  "v_cndmask_b32 v108, v30, v108, s[86:87]\n"
  "v_cmp_lt_i32 s[72:73], 24, v21\n"
  "v_cndmask_b32 v101, v30, v101, s[88:89]\n"
  "v_cndmask_b32 v109, v30, v109, s[88:89]\n"
  "v_cmp_lt_i32 s[86:87], 28, v21\n"
  "v_cndmask_b32 v102, v30, v102, s[72:73]\n"
  "v_cndmask_b32 v110, v30, v110, s[72:73]\n"
  "v_cmp_lt_i32 s[88:89], 32, v21\n"
  "v_cndmask_b32 v103, v30, v103, s[86:87]\n"
  "v_cndmask_b32 v111, v30, v111, s[86:87]\n"
  "v_cmp_lt_i32 s[72:73], 36, v21\n"
  "v_cndmask_b32 v112, v30, v112, s[88:89]\n"
  "v_cndmask_b32 v120, v30, v120, s[88:89]\n"
  "v_cmp_lt_i32 s[86:87], 40, v21\n"
  "v_cndmask_b32 v113, v30, v113, s[72:73]\n"
  "v_cndmask_b32 v121, v30, v121, s[72:73]\n"
  "v_cmp_lt_i32 s[88:89], 44, v21\n"
  "v_cndmask_b32 v114, v30, v114, s[86:87]\n"
  "v_cndmask_b32 v122, v30, v122, s[86:87]\n"
  "v_cmp_lt_i32 s[72:73], 48, v21\n"
  "v_cndmask_b32 v115, v30, v115, s[88:89]\n"
  "v_cndmask_b32 v123, v30, v123, s[88:89]\n"
  "v_cmp_lt_i32 s[86:87], 52, v21\n"
  "v_cndmask_b32 v116, v30, v116, s[72:73]\n"
  "v_cndmask_b32 v124, v30, v124, s[72:73]\n"
  "v_cmp_lt_i32 s[88:89], 56, v21\n"
  "v_cndmask_b32 v117, v30, v117, s[86:87]\n"
  "v_cndmask_b32 v125, v30, v125, s[86:87]\n"
  "v_cmp_lt_i32 s[72:73], 60, v21\n"
  "v_cndmask_b32 v118, v30, v118, s[88:89]\n"
  "v_cndmask_b32 v126, v30, v126, s[88:89]\n"
  "v_cndmask_b32 v119, v30, v119, s[72:73]\n"
  "v_cndmask_b32 v127, v30, v127, s[72:73]\n"
  "s_nop 1\n"
  "s_setpc_b64 s[70:71]\n"
  "Lidle%=:\n"
  "v_readlane_b32 s17, v120, s68\n"
  "v_readlane_b32 s18, v121, s68\n"
  "s_mov_b32 s15, 0\n"
  "v_mov_b32 v14, 0\n"
  "s_lshl2_add_u32 s98, s92, 147456\n"
  "v_mov_b32 v15, s98\n"
  "ds_write_b32 v15, v14\n"
  "s_lshl_b32 s67, s95, 10\n"
  "s_add_u32 s67, s67, 17301504\n"
  "s_waitcnt vmcnt(0)\n"
  "v_mov_b64 v[8:9], v[26:27]\n"
  "s_cmp_eq_u32 s22, 0\n"
  "s_cbranch_scc1 Lprep%=\n"
  "s_cmp_eq_u32 s11, 0\n"
  "s_cselect_b32 s37, 0, 0xf0000000\n"
  "v_add_u32 v62, 1, v26\n"
  "v_xor_b32 v63, 0x40000000, v27\n"
  "v_mov_b32 v61, s37\n"
  "s_mov_b64 exec, 1\n"
  "buffer_store_dwordx2 v[62:63], v61, s[56:59], s36 offen offset:512 sc1\n"
  "s_mov_b64 exec, -1\n"
  "s_branch Lprep%=\n"
  "Lend%=:\n"
      : "={s[12:19]}"(so), "={s[40:55]}"(kp), "={s[56:63]}"(kb), "+{s10}"(g), "+{v1}"(tid)
      : "{s[8:9]}"(ka), "{s11}"(pass), "{v[2:5]}"(mg)
      : "v8", "v9", "v10", "v11", "v12", "v13", "v14", "v15", "v16", "v17", "v18", "v19", "v20", "v21", "v22", "v23", "v24", "v25", "v26", "v27", "v28", "v29", "v30", "v31", "v32", "v33", "v34", "v35", "v36", "v37", "v38", "v39", "v40", "v41", "v42", "v43", "v44", "v45", "v46", "v47", "v48", "v49", "v50", "v51", "v52", "v53", "v54", "v55", "v56", "v57", "v58", "v59", "v60", "v61", "v62", "v63", "v64", "v65", "v66", "v67", "v68", "v69", "v70", "v71", "v72", "v73", "v74", "v75", "v76", "v77", "v78", "v79", "v80", "v81", "v82", "v83", "v84", "v85", "v86", "v87", "v88", "v89", "v90", "v91", "v92", "v93", "v94", "v95", "v96", "v97", "v98", "v99", "v100", "v101", "v102", "v103", "v104", "v105", "v106", "v107", "v108", "v109", "v110", "v111", "v112", "v113", "v114", "v115", "v116", "v117", "v118", "v119", "v120", "v121", "v122", "v123", "v124", "v125", "v126", "v127", "v128", "v129", "v130", "v131", "v132", "v133", "v134", "v135", "v136", "v137", "v138", "v139", "v140", "v141", "v142", "v143", "v144", "v145", "v146", "v147", "v148", "v149", "v150", "v151", "v152", "v153", "v154", "v155", "v156", "v157", "v158", "v159", "v160", "v161", "v162", "v163", "v164", "v165", "v166", "v167", "v168", "v169", "v170", "v171", "v172", "v173", "v174", "v175", "v176", "v177", "v178", "v179", "v180", "v181", "v182", "v183", "v184", "v185", "v186", "v187", "v188", "v189", "v190", "v191", "v192", "v193", "v194", "v195", "v196", "v197", "v198", "v199", "v200", "v201", "v202", "v203", "v204", "v205", "v206", "v207", "v208", "v209", "v210", "v211", "v212", "v213", "v214", "v215", "v216", "v217", "v218", "v219", "v220", "v221", "v222", "v223", "v224", "v225", "v226", "v227", "v228", "v229", "v230", "v231", "v232", "v233", "v234", "v235", "v236", "v237", "v238", "v239", "v240", "v241", "v242", "v243", "v244", "v245", "v246", "v247", "v248", "v249", "v250", "v251", "v252", "v253", "v254", "v255", "a0", "a1", "a2", "a3", "a4", "a5", "a6", "a7", "a8", "a9", "a10", "a11", "a12", "a13", "a14", "a15", "a16", "a17", "a18", "a19", "a20", "a21", "a22", "a23", "a24", "a25", "a26", "a27", "a28", "a29", "a30", "a31", "a32", "a33", "a34", "a35", "a36", "a37", "a38", "a39", "a40", "a41", "a42", "a43", "a44", "a45", "a46", "a47", "a48", "a49", "a50", "a51", "a52", "a53", "a54", "a55", "a56", "a57", "a58", "a59", "a60", "a61", "a62", "a63", "a64", "a65", "a66", "a67", "a68", "a69", "a70", "a71", "a72", "a73", "a74", "a75", "a76", "a77", "a78", "a79", "a80", "a81", "a82", "a83", "a84", "a85", "a86", "a87", "a88", "a89", "a90", "a91", "a92", "a93", "a94", "a95", "a96", "a97", "a98", "a99", "a100", "a101", "a102", "a103", "a104", "a105", "a106", "a107", "a108", "a109", "a110", "a111", "a112", "a113", "a114", "a115", "a116", "a117", "a118", "a119", "a120", "a121", "a122", "a123", "a124", "a125", "a126", "a127", "a128", "a129", "a130", "a131", "a132", "a133", "a134", "a135", "a136", "a137", "a138", "a139", "a140", "a141", "a142", "a143", "a144", "a145", "a146", "a147", "a148", "a149", "a150", "a151", "a152", "a153", "a154", "a155", "a156", "a157", "a158", "a159", "a160", "a161", "a162", "a163", "a164", "a165", "a166", "a167", "a168", "a169", "a170", "a171", "a172", "a173", "a174", "a175", "a176", "a177", "a178", "a179", "a180", "a181", "a182", "a183", "a184", "a185", "a186", "a187", "a188", "a189", "a190", "a191", "a192", "a193", "a194", "a195", "a196", "a197", "a198", "a199", "s20", "s21", "s22", "s23", "s24", "s25", "s26", "s27", "s28", "s29", "s30", "s31", "s36", "s37", "s38", "s39", "s64", "s65", "s66", "s67", "s68", "s69", "s70", "s71", "s72", "s73", "s74", "s75", "s76", "s77", "s78", "s79", "s80", "s81", "s82", "s83", "s84", "s85", "s86", "s87", "s88", "s89", "s90", "s91", "s92", "s93", "s94", "s95", "s96", "s97", "s98", "s99", "vcc", "scc", "memory");
    const unsigned lane = tid & 63u, wid = rfl(tid >> 6);
    if (__builtin_expect(so[0] == 0u, 1)) return;
    if (so[0] == 2u) break;
    asm volatile("s_waitcnt vmcnt(0)" ::: "memory");
    {
      const unsigned b = so[1], t0 = so[2], t1 = so[3], len = so[4], kvh = KVH(g);
      const int* bt = mkptr<const int>(kp[0], kp[1]); const int* lens = mkptr<const int>(kb[4], kb[5]);
      const char* Kc = mkptr<const char>(kp[4], kp[5]); const char* Q = mkptr<const char>(kp[12], kp[13]);
      const float qs = lens[b] <= 0 ? 0.f : QSCALE;
      const char* Qg = (const char*)Q + (size_t)(b * HKV + kvh) * 65536u;
      const unsigned l32b = lane & 31u, hib = lane >> 5;
#pragma unroll 1
      for (unsigned qb = 0; qb < 2; ++qb) {
        bf16x8 qf[8];
#pragma unroll
        for (int d0 = 0; d0 < 8; ++d0) {
          const u32x4 w = *(const u32x4*)(Qg + (wid * 64u + qb * 32u + l32b) * 256u + d0 * 32u + hib * 16u); u32x4 o;
#pragma unroll
          for (int e = 0; e < 4; ++e) o[e] = cvtpk(bflo(w[e]) * qs, bfhi(w[e]) * qs);
          qf[d0] = __builtin_bit_cast(bf16x8, o);
        }
        float m = -__builtin_inff();
#pragma unroll 1
        for (unsigned i = 0; i < (t1 - t0) * 2u; ++i) {
          const unsigned kb = ((g >> 5) < FIX_WGS && i < 2u * FIX_TILES ? (g >> 5) * FIX_TILES : t0) * 2u + i;
          const unsigned key = kb * 32u + l32b;
          const unsigned pg = (unsigned)bt[b * NBPS + (key >> 4)];
          const char* kr = (const char*)Kc + ((size_t)pg * PAGE + (key & 15u)) * (HKV * D * 2) + kvh * 256u + hib * 16u;
          f32x16 s = {};
#pragma unroll
          for (int d0 = 0; d0 < 8; ++d0) s = __builtin_amdgcn_mfma_f32_32x32x16_bf16(*(const bf16x8*)(kr + d0 * 32), qf[d0], s, 0, 0, 0);
#pragma unroll
          for (int r = 0; r < 16; ++r) { const unsigned kk = kb * 32u + (r & 3) + 8 * (r >> 2) + 4u * hib; m = kk < len ? fmaxf(m, s[r]) : m; }
        }
        auto pr = __builtin_amdgcn_permlane32_swap(__float_as_uint(m), __float_as_uint(m), false, false);
        m = fmaxf(__uint_as_float(pr[0]), __uint_as_float(pr[1]));
        const float ma = __shfl(m, (int)(lane & 15u)), mb = __shfl(m, (int)(16u + (lane & 15u)));
        if (qb == 0) { mg[0] = ma; mg[1] = mb; } else { mg[2] = ma; mg[3] = mb; }
      }
      asm volatile("s_barrier" ::: "memory");
    }
  }
  const unsigned lane = tid & 63u, wid = rfl(tid >> 6);
  const unsigned n = so[5], start = so[6], slot = so[7], kvh = KVH(g);
  char* ws = mkptr<char>(kb[0], kb[1]); unsigned short* out = mkptr<unsigned short>(kb[6], kb[7]);
  __amdgpu_buffer_rsrc_t rp = __builtin_amdgcn_make_buffer_rsrc(ws + (size_t)(kvh * 64u + start) * PART_STRIDE, (short)0, n * PART_STRIDE, 0x00020000);
  unsigned short* og = out + (size_t)((slot >> 3) * HKV + kvh) * 32768u;
  const unsigned vd_ = lane * 16u, vm_ = 65536u + (lane & 15u) * 8u;
#pragma unroll 1
  for (unsigned u = (slot & 7u) * 8u + wid * 2u, ue = u + 2u; u < ue; ++u) {
    float M = -__builtin_inff(), L = 0.f, o[8] = {};
#pragma unroll 1
    for (unsigned s0 = 0; s0 < n; s0 += 4u) {
      u32x2 ml[4]; u32x4 x[4];
#pragma unroll
      for (int s = 0; s < 4; ++s) {
        ml[s] = __builtin_amdgcn_raw_buffer_load_b64(rp, vm_ + (u >> 2) * 128u, (s0 + s) * PART_STRIDE, SC1);
        x[s] = __builtin_amdgcn_raw_buffer_load_b128(rp, vd_ + u * 1024u, (s0 + s) * PART_STRIDE, SC1);
      }
      float Mn = M;
#pragma unroll
      for (int s = 0; s < 4; ++s) if (s0 + s < n) Mn = fmaxf(Mn, __uint_as_float(ml[s][0]));
      const float a = __builtin_amdgcn_exp2f(M - Mn);
      L *= a;
#pragma unroll
      for (int e = 0; e < 8; ++e) o[e] *= a;
      M = Mn;
#pragma unroll
      for (int s = 0; s < 4; ++s) if (s0 + s < n) {
        const float w = __builtin_amdgcn_exp2f(__uint_as_float(ml[s][0]) - M);
        L += w * __uint_as_float(ml[s][1]);
#pragma unroll
        for (int e = 0; e < 4; ++e) { o[2 * e] += w * bflo(x[s][e]); o[2 * e + 1] += w * bfhi(x[s][e]); }
      }
    }
    store_unit(o, L, u, lane, og);
  }
}

extern "C" void kernel_launch(void* const* d_in, const int* in_sizes, int n_in, void* d_out, int out_size, void* d_ws, size_t ws_size, hipStream_t stream) {
  const unsigned long long FL = 0x00020000ull << 32;
  (void)hipFuncSetAttribute((const void*)attn_main, hipFuncAttributeMaxDynamicSharedMemorySize, (int)LDS_BYTES);
  hipLaunchKernelGGL(attn_main, dim3(256), dim3(256), LDS_BYTES, stream,
                     (const int*)d_in[3], FL | (unsigned)(NB * NBPS * 4), (const unsigned short*)d_in[1], FL | ((unsigned)in_sizes[1] * 2u),
                     (const unsigned short*)d_in[2], FL | ((unsigned)in_sizes[2] * 2u), (const unsigned short*)d_in[0], FL | ((unsigned)in_sizes[0] * 2u),
                     (char*)(((size_t)d_ws + ws_size - WS_BYTES) & ~(size_t)0xffffu), FL | WS_BYTES, (const int*)d_in[4], (unsigned short*)d_out);
}
```
